```python
import torch

def _softmax(x, axis=-1):
    return torch.softmax(x, dim=axis)

def _gelu(x):
    return torch.nn.functional.gelu(x, approximate='tanh')

def _silu(x):
    return torch.nn.functional.silu(x)

def _sigmoid(x):
    return torch.sigmoid(x)

def _layernorm(x, axis=-1, eps=1e-05):
    m = x.mean(dim=axis, keepdim=True)
    v = x.var(dim=axis, keepdim=True, unbiased=False)
    return (x - m) / torch.sqrt(v + eps)

def _scatter_add(tgt, idx, src, axis=0):
    return tgt.clone().index_add_(axis, idx, src)

def _segment_sum(src, seg, n):
    out = torch.zeros((n,) + src.shape[1:], dtype=src.dtype)
    return out.index_add_(0, seg, src)

def _pad(x, pw):
    flat = tuple((v for pair in reversed(pw) for v in pair))
    return torch.nn.functional.pad(x, flat)
kernel_name = 'gated_mix__memory_aug_recurrent_mem_tr__ssm_conv_s4__prompt'

def setup_inputs():

    _w = lambda a: (lambda t: t.to(torch.bfloat16) if t.is_floating_point() else t)(torch.as_tensor(a))

    def _A_setup():
        B, S, Dm = (1, 2048, 640)
        H, Dh = (32, 128)
        M, seg = (8, 32)
        seg = min(seg, S)
        S = S // seg * seg
        return {'x': _w((lambda *s: torch.randn(*s, dtype=torch.bfloat16) * 0.05)(B, S, Dm)), 'mem0': _w((lambda *s: torch.randn(*s, dtype=torch.bfloat16) * 0.4)(B, M, Dm)), 'Wq': _w((lambda *s: torch.randn(*s, dtype=torch.bfloat16) * 6.4)(Dm, H, Dh) * Dm ** (-0.5)), 'Wk': _w((lambda *s: torch.randn(*s, dtype=torch.bfloat16) * 6.4)(Dm, H, Dh) * Dm ** (-0.5)), 'Wv': _w((lambda *s: torch.randn(*s, dtype=torch.bfloat16) * 0.4)(Dm, H, Dh) * Dm ** (-0.5)), 'Wo': _w((lambda *s: torch.randn(*s, dtype=torch.bfloat16) * 0.4)(H, Dh, Dm) * (H * Dh) ** (-0.5)), 'seg_len': seg}
    _A_in = _A_setup()

    def _B_setup():
        B, L, D, N = (1, 2048, 640, 128)
        A_re = -(lambda _x: torch.exp(torch.as_tensor(_x)))((lambda *s: torch.randn(*s, dtype=torch.bfloat16) * 6.4)(D, N))
        A_im = torch.pi * (lambda *s: torch.randn(*s, dtype=torch.bfloat16) * 0.05)(D, N)
        B_re = (lambda *s: torch.randn(*s, dtype=torch.bfloat16) * 0.1)(D, N)
        B_im = (lambda *s: torch.randn(*s, dtype=torch.bfloat16) * 0.1)(D, N)
        C_re = (lambda *s: torch.randn(*s, dtype=torch.bfloat16) * 0.1)(D, N)
        C_im = (lambda *s: torch.randn(*s, dtype=torch.bfloat16) * 0.1)(D, N)
        log_dt = (lambda *s: torch.randn(*s, dtype=torch.bfloat16) * 0.05)(D) * 0.5 - 4.0
        Dskip = (lambda *s: torch.randn(*s, dtype=torch.bfloat16) * 0.05)(D)
        return {'u': _w((lambda *s: torch.randn(*s, dtype=torch.bfloat16) * 0.05)(B, L, D)), 'A_re': _w(A_re), 'A_im': _w(A_im), 'B_re': _w(B_re), 'B_im': _w(B_im), 'C_re': _w(C_re), 'C_im': _w(C_im), 'log_dt': _w(log_dt), 'Dskip': _w(Dskip)}
    _B_in = _B_setup()
    return {'A_x': _A_in['x'], 'A_mem0': _A_in['mem0'], 'A_Wq': _A_in['Wq'], 'A_Wk': _A_in['Wk'], 'A_Wv': _A_in['Wv'], 'A_Wo': _A_in['Wo'], 'A_seg_len': _A_in['seg_len'], 'B_u': _B_in['u'], 'B_A_re': _B_in['A_re'], 'B_A_im': _B_in['A_im'], 'B_B_re': _B_in['B_re'], 'B_B_im': _B_in['B_im'], 'B_C_re': _B_in['C_re'], 'B_C_im': _B_in['C_im'], 'B_log_dt': _B_in['log_dt'], 'B_Dskip': _B_in['Dskip'], 'gate_w': _w((lambda *s: torch.randn(*s, dtype=torch.bfloat16) * 0.05)(640) * 16.0)}

def reference(A_x, A_mem0, A_Wq, A_Wk, A_Wv, A_Wo, A_seg_len, B_u, B_A_re, B_A_im, B_B_re, B_B_im, B_C_re, B_C_im, B_log_dt, B_Dskip, gate_w):

    def _A_ref(x, mem0, Wq, Wk, Wv, Wo, seg_len):
        B, S, Dm = x.shape
        H, Dh = (Wq.shape[1], Wq.shape[2])
        M = mem0.shape[1]
        scale = Dh ** (-0.5)
        n_seg = S // seg_len
        L = M + seg_len
        idx = torch.arange(L)
        mask = (idx[:, None] < M) | (idx[:, None] >= idx[None, :])
        mem = mem0
        outs = []
        for s in range(n_seg):
            xs = x[:, s * seg_len:(s + 1) * seg_len, :].clone()
            h = torch.concatenate([mem, xs], axis=1)
            q = torch.einsum('btd,dhe->bhte', h, Wq)
            k = torch.einsum('btd,dhe->bhte', h, Wk)
            v = torch.einsum('btd,dhe->bhte', h, Wv)
            scores = torch.einsum('bhqe,bhke->bhqk', q, k) * scale
            scores = torch.where(mask[None, None], scores, -1000000000.0)
            p = _softmax(scores, axis=-1)
            o = torch.einsum('bhqk,bhke->bhqe', p, v)
            o = torch.einsum('bhte,hed->btd', o, Wo)
            mem = o[:, :M, :].clone()
            outs.append(o[:, M:, :])
        return torch.concatenate(outs, axis=1)

    def _B_ref(u, A_re, A_im, B_re, B_im, C_re, C_im, log_dt, Dskip):
        Bsz, L, D = u.shape
        dt = torch.exp(log_dt)[:, None]
        A = A_re + 1j * A_im
        Bc = B_re + 1j * B_im
        Cc = C_re + 1j * C_im
        logdA = dt * A
        dB = dt * Bc
        l = (lambda x, dt: x.to(dt))(torch.arange(L), A_re.dtype)
        P = torch.exp(logdA[:, :, None] * l[None, None, :])
        K = torch.einsum('dn,dnl,dn->dl', Cc, P, dB).real
        L2 = 2 * L
        Kp = _pad(K, ((0, 0), (0, L)))
        up = (lambda x, *d: x.permute(*d))(u, 0, 2, 1)
        up = _pad(up, ((0, 0), (0, 0), (0, L)))
        Kf = (lambda x, axes=(-2, -1): torch.fft.rfft2(x.float(), dim=axes))(Kp[:, None, :], axes=(-2, -1))
        Uf = (lambda x, axes=(-2, -1): torch.fft.rfft2(x.float(), dim=axes))(up[:, :, None, :], axes=(-2, -1))
        Yf = Uf * Kf[None, :, :, :]
        y = (lambda x, s, axes=(-2, -1): torch.fft.irfft2(x, s=s, dim=axes).to(torch.bfloat16))(Yf, (1, L2), axes=(-2, -1))[:, :, 0, :L]
        y = (lambda x, *d: x.permute(*d))(y, 0, 2, 1) + u * Dskip[None, None, :]
        return y
    x = A_x
    g = _sigmoid((x * gate_w).sum(-1, keepdims=True))
    a = _A_ref(x, A_mem0, A_Wq, A_Wk, A_Wv, A_Wo, A_seg_len)
    b = _B_ref(x, B_A_re, B_A_im, B_B_re, B_B_im, B_C_re, B_C_im, B_log_dt, B_Dskip)
    return g * a + (1 - g) * b
```

```cpp
#include <hip/hip_runtime.h>
#include <hip/hip_bf16.h>
#include <stdint.h>

typedef __bf16 bf16;
typedef bf16 bf16x2 __attribute__((ext_vector_type(2)));
typedef bf16 bf16x4 __attribute__((ext_vector_type(4)));
typedef bf16 bf16x8 __attribute__((ext_vector_type(8)));
typedef bf16 bf16x16 __attribute__((ext_vector_type(16)));
typedef float f32x2 __attribute__((ext_vector_type(2)));
typedef float f32x4 __attribute__((ext_vector_type(4)));
typedef unsigned u32x2 __attribute__((ext_vector_type(2)));
typedef unsigned u32x4 __attribute__((ext_vector_type(4)));
#define LAS __attribute__((address_space(3)))
#define GAS __attribute__((address_space(1)))
#define DEV __device__ __forceinline__

constexpr int SEQ = 2048, DM = 640, NH = 32, NSEG = 64;
constexpr unsigned SENT = 0xFFFFFFFFu;

constexpr unsigned MEM_OFF = 0, MEM_B = 12288;
constexpr unsigned P_OFF = 24576, P_B = 20480;
constexpr unsigned KM_OFF = 65536;
constexpr unsigned Y_OFF = 131072;
constexpr size_t XCH_BYTES = (size_t)66 << 20;
constexpr size_t QF_OFF = (size_t)68 << 20;
constexpr size_t KF_OFF = QF_OFF + ((size_t)16 << 20);
constexpr size_t UF_OFF = KF_OFF + ((size_t)16 << 20);
constexpr size_t PX_OFF = UF_OFF + ((size_t)80 << 20);
constexpr size_t PM_OFF = PX_OFF + ((size_t)4 << 20);
constexpr size_t BM_OFF = PM_OFF + ((size_t)1 << 20);
constexpr size_t G_OFF = BM_OFF + ((size_t)3 << 20);
constexpr size_t CTL_OFF = G_OFF + ((size_t)1 << 16);

struct Params {
  const bf16 *x, *mem0, *Wq, *Wk, *Wv, *Wo;
  const bf16 *Are, *Aim, *Bre, *Bim, *Cre, *Cim, *logdt, *Dskip, *gatew;
  bf16* out;
  char* ws;
  char* xch;
};

DEV unsigned mapoff(unsigned o) { return (o & 8191u) | ((o >> 13) << 14); }
DEV unsigned slotbase(int s) { return __builtin_amdgcn_readfirstlane((unsigned)s << 20); }

DEV f32x4 mfma16(bf16x8 a, bf16x8 b, f32x4 c) { return __builtin_amdgcn_mfma_f32_16x16x32_bf16(a, b, c, 0, 0, 0); }
DEV f32x4 smfma(bf16x8 a, bf16x16 b, f32x4 c, int idx) { return __builtin_amdgcn_smfmac_f32_16x16x64_bf16(a, b, c, idx, 0, 0); }
DEV bf16x8 wlo(bf16x16 w) { return __builtin_shufflevector(w, w, 0, 1, 2, 3, 4, 5, 6, 7); }
DEV bf16x8 whi(bf16x16 w) { return __builtin_shufflevector(w, w, 8, 9, 10, 11, 12, 13, 14, 15); }
DEV unsigned pk2(float a, float b) { f32x2 f = {a, b}; return __builtin_bit_cast(unsigned, __builtin_convertvector(f, bf16x2)); }
DEV float bfr(float a) { return (float)(bf16)a; }
DEV float bflo(unsigned u) { return __builtin_bit_cast(float, u << 16); }
DEV float bfhi(unsigned u) { return __builtin_bit_cast(float, u & 0xffff0000u); }
DEV unsigned nosent(unsigned u) { return u == SENT ? 0x7fc07fc0u : u; }
DEV bf16x4 trread(unsigned ldsaddr) { return __builtin_amdgcn_ds_read_tr16_b64_v4bf16((LAS bf16x4*)(uintptr_t)ldsaddr); }
DEV bf16x8 cat4(bf16x4 a, bf16x4 b) { return __builtin_shufflevector(a, b, 0, 1, 2, 3, 4, 5, 6, 7); }
DEV bf16x16 cat8(bf16x8 a, bf16x8 b) { return __builtin_shufflevector(a, b, 0, 1, 2, 3, 4, 5, 6, 7, 8, 9, 10, 11, 12, 13, 14, 15); }
DEV bf16x8 ldsread16(unsigned a) { return *(LAS bf16x8*)(uintptr_t)a; }
DEV void ldswrite16(unsigned a, u32x4 v) { *(LAS u32x4*)(uintptr_t)a = v; }
DEV void ldswrite8(unsigned a, u32x2 v) { *(LAS u32x2*)(uintptr_t)a = v; }
DEV void glds16(const void* gsrc, unsigned lds_dst) {
  unsigned keep;
  asm volatile("s_mov_b32 %0, m0\n\ts_mov_b32 m0, %2\n\ts_nop 0\n\tglobal_load_lds_dwordx4 %1, off\n\ts_mov_b32 m0, %0"
               : "=&s"(keep) : "v"(gsrc), "s"(lds_dst) : "memory");
}
#define WAIT_VM(n) asm volatile("s_waitcnt vmcnt(" #n ")" ::: "memory")
#define WAIT_LGKM0() asm volatile("s_waitcnt lgkmcnt(0)" ::: "memory")
#define BAR() do { asm volatile("s_waitcnt lgkmcnt(0)" ::: "memory"); __builtin_amdgcn_s_barrier(); asm volatile("" ::: "memory"); } while (0)
DEV unsigned umax4(u32x4 v) { return max(max(v.x, v.y), max(v.z, v.w)); }
DEV __amdgpu_buffer_rsrc_t mkrsrc(const void* p) { return __builtin_amdgcn_make_buffer_rsrc((void*)p, 0, 0x7fffffff, 0x00020000); }
DEV u32x4 bld16(__amdgpu_buffer_rsrc_t r, unsigned vo, unsigned so) { return __builtin_amdgcn_raw_buffer_load_b128(r, vo, so, 16); }
DEV void bst16(u32x4 v, __amdgpu_buffer_rsrc_t r, unsigned vo, unsigned so) { __builtin_amdgcn_raw_buffer_store_b128(v, r, vo, so, 16); }
DEV void bst8(u32x2 v, __amdgpu_buffer_rsrc_t r, unsigned vo, unsigned so) { __builtin_amdgcn_raw_buffer_store_b64(v, r, vo, so, 16); }

template <int ROLE>
DEV void load_weights(const Params& p, int h, unsigned lb, int w, int lane, bf16x16 (&W)[20]) {
  auto issue = [&](int r) {
#pragma unroll
    for (int i = 0; i < 8; ++i) {
      int q = w * 8 + i;
      int T = r * 4 + (q >> 4);
      int row = 4 * (q & 15) + (lane >> 4);
      int slot = lane & 15;
      int f = (row & 3) | (((row >> 3) & 1) << 2);
      int col = (((slot >> 1) ^ f) << 4) + ((slot & 1) << 3);
      const bf16* src;
      if (ROLE == 0) {
        src = ((T & 1) ? p.Wk : p.Wq) + (size_t)(64 * (T >> 1) + row) * 4096 + h * 128 + col;
      } else {
        if (T < 10) src = p.Wv + (size_t)(64 * T + row) * 4096 + h * 128 + col;
        else { int u = T - 10; src = p.Wo + (size_t)(h * 128 + 64 * (u & 1) + row) * 640 + (u >> 1) * 128 + col; }
      }
      glds16(src, __builtin_amdgcn_readfirstlane(lb + (r & 1) * 65536 + q * 1024));
    }
  };
  const int bg = lane >> 4, qq = (lane & 15) >> 2, pp = lane & 3;
  const int fx = qq | ((bg & 1) << 2);
  unsigned a0, a1;
  if (ROLE == 0) {
    unsigned tb = (w >> 2) * 16384 + (8 * bg + qq) * 256 + pp * 8;
    a0 = tb + (((2 * (w & 3)) ^ fx) << 5);
    a1 = tb + (((2 * (w & 3) + 1) ^ fx) << 5);
  } else {
    unsigned tb = (8 * bg + qq) * 256 + pp * 8 + ((w ^ fx) << 5);
    a0 = tb; a1 = tb + 16384;
  }
  issue(0); issue(1);
#pragma unroll
  for (int r = 0; r < 5; ++r) {
    if (r < 4) WAIT_VM(8); else WAIT_VM(0);
    BAR();
#pragma unroll
    for (int pr = 0; pr < 2; ++pr) {
      unsigned b0 = lb + (r & 1) * 65536 + pr * 32768 + a0, b1 = lb + (r & 1) * 65536 + pr * 32768 + a1;
      W[(r * 2 + pr) * 2 + 0] = cat8(cat4(trread(b0), trread(b0 + 1024)), cat4(trread(b0 + 8192), trread(b0 + 9216)));
      W[(r * 2 + pr) * 2 + 1] = cat8(cat4(trread(b1), trread(b1 + 1024)), cat4(trread(b1 + 8192), trread(b1 + 9216)));
    }
    BAR();
    if (r + 2 < 5) issue(r + 2);
  }
}

DEV void stage_x(const Params& p, int s, unsigned dst, int w, int lane) {
#pragma unroll
  for (int i = 0; i < 5; ++i) {
    int q = w * 5 + i;
    int tt = q / 20, kb = q % 20;
    const bf16* src = p.x + (size_t)(32 * s + 16 * tt + (lane & 15)) * DM + 32 * kb + 8 * (lane >> 4);
    glds16(src, __builtin_amdgcn_readfirstlane(dst + q * 1024));
  }
}

DEV void worker_qk(const Params& p, int h, int s0, int sstep, unsigned lb, int w, int lane) {
  bf16x16 W[20];
  load_weights<0>(p, h, lb, w, lane, W);
  char* dstb = p.ws + ((w >> 2) ? KF_OFF : QF_OFF);
  stage_x(p, s0, lb, w, lane);
  int buf = 0;
  for (int s = s0; s < NSEG; s += sstep, buf ^= 1) {
    WAIT_VM(0);
    BAR();
    if (s + sstep < NSEG) stage_x(p, s + sstep, lb + (buf ^ 1) * 40960, w, lane);
    unsigned xb = lb + buf * 40960 + lane * 16;
    f32x4 acc[2][2] = {};
#pragma unroll
    for (int kp = 0; kp < 10; ++kp) {
#pragma unroll
      for (int hf = 0; hf < 2; ++hf) {
        bf16x8 x0 = ldsread16(xb + (2 * kp + hf) * 1024), x1 = ldsread16(xb + (20 + 2 * kp + hf) * 1024);
        bf16x8 w0 = hf ? whi(W[2 * kp]) : wlo(W[2 * kp]), w1 = hf ? whi(W[2 * kp + 1]) : wlo(W[2 * kp + 1]);
        acc[0][0] = mfma16(w0, x0, acc[0][0]);
        acc[1][0] = mfma16(w1, x0, acc[1][0]);
        acc[0][1] = mfma16(w0, x1, acc[0][1]);
        acc[1][1] = mfma16(w1, x1, acc[1][1]);
      }
    }
#pragma unroll
    for (int tt = 0; tt < 2; ++tt) {
      u32x4 o = {pk2(acc[0][tt][0], acc[0][tt][1]), pk2(acc[0][tt][2], acc[0][tt][3]), pk2(acc[1][tt][0], acc[1][tt][1]), pk2(acc[1][tt][2], acc[1][tt][3])};
      *(u32x4*)(dstb + ((size_t)((s * 32 + h) * 2 + tt) * 4 + (w & 3)) * 1024 + lane * 16) = o;
    }
  }
}

DEV void worker_vo(const Params& p, int h, int s0, int sstep, unsigned lb, int w, int lane) {
  bf16x16 W[20];
  load_weights<1>(p, h, lb, w, lane, W);
  stage_x(p, s0, lb, w, lane);
  const unsigned vf = lb + 81920;
  const int g = lane >> 4;
  const unsigned vw = vf + (w >> 1) * 1024 + ((lane & 15) + 16 * (2 * (w & 1) + (g >> 1))) * 16 + (g & 1) * 8;
  int buf = 0;
  for (int s = s0; s < NSEG; s += sstep, buf ^= 1) {
    WAIT_VM(0);
    BAR();
    if (s + sstep < NSEG) stage_x(p, s + sstep, lb + (buf ^ 1) * 40960, w, lane);
    unsigned xb = lb + buf * 40960 + lane * 16;
    f32x4 a0 = {}, a1 = {};
#pragma unroll
    for (int kp = 0; kp < 10; ++kp) {
#pragma unroll
      for (int hf = 0; hf < 2; ++hf) {
        bf16x8 x0 = ldsread16(xb + (2 * kp + hf) * 1024), x1 = ldsread16(xb + (20 + 2 * kp + hf) * 1024);
        bf16x8 w0 = hf ? whi(W[kp]) : wlo(W[kp]);
        a0 = mfma16(w0, x0, a0);
        a1 = mfma16(w0, x1, a1);
      }
    }
    ldswrite8(vw, (u32x2){pk2(a0[0], a0[1]), pk2(a0[2], a0[3])});
    ldswrite8(vw + 4096, (u32x2){pk2(a1[0], a1[1]), pk2(a1[2], a1[3])});
    BAR();
    bf16x8 V[2][4];
#pragma unroll
    for (int tt = 0; tt < 2; ++tt)
#pragma unroll
      for (int kk = 0; kk < 4; ++kk) V[tt][kk] = ldsread16(vf + (tt * 4 + kk) * 1024 + lane * 16);
#pragma unroll
    for (int c = 0; c < 5; ++c) {
      f32x4 u0 = {}, u1 = {};
#pragma unroll
      for (int kk = 0; kk < 4; ++kk) {
        bf16x8 wb = (kk & 1) ? whi(W[10 + 2 * c + (kk >> 1)]) : wlo(W[10 + 2 * c + (kk >> 1)]);
        u0 = mfma16(V[0][kk], wb, u0);
        u1 = mfma16(V[1][kk], wb, u1);
      }
      u32x4 o = {pk2(u0[0], u0[1]), pk2(u0[2], u0[3]), pk2(u1[0], u1[1]), pk2(u1[2], u1[3])};
      *(u32x4*)(p.ws + UF_OFF + ((size_t)(s * 40 + 8 * c + w) * 32 + h) * 1024 + lane * 16) = o;
    }
  }
}

__global__ void __launch_bounds__(512, 2) k_pre(Params p) {
  extern __shared__ __attribute__((aligned(16))) char lds[];
  const unsigned lb = (unsigned)(uintptr_t)(LAS char*)lds;
  const int b = blockIdx.x, lane = threadIdx.x & 63, w = __builtin_amdgcn_readfirstlane(threadIdx.x >> 6);
  const int id = b & 63, set = b >> 6;
  if (id < 32) worker_qk(p, id, set, 4, lb, w, lane);
  else worker_vo(p, id - 32, set, 4, lb, w, lane);
}

constexpr int SPIN_CAP = 1 << 20;
template <int N>
DEV void gather(__amdgpu_buffer_rsrc_t rs, const unsigned (&off)[N], unsigned sb, u32x4 (&v)[N]) {
  for (int spins = 0; spins < SPIN_CAP; ++spins) {
    asm volatile("" ::: "memory");
#pragma unroll
    for (int i = 0; i < N; ++i) v[i] = bld16(rs, off[i], sb);
    unsigned m = 0;
#pragma unroll
    for (int i = 0; i < N; ++i) m = max(m, umax4(v[i]));
    if (__builtin_amdgcn_ballot_w64(m == SENT) == 0) break;
  }
}
DEV void swapadd(float& a, float& b, float& s) {
  auto r = __builtin_amdgcn_permlane32_swap(__builtin_bit_cast(unsigned, a), __builtin_bit_cast(unsigned, b), false, false);
  s = __builtin_bit_cast(float, (unsigned)r[0]) + __builtin_bit_cast(float, (unsigned)r[1]);
}
DEV float xor16(float v) { return __shfl_xor(v, 16); }
DEV float xor32(float v) { return __shfl_xor(v, 32); }

DEV void gather_mem(__amdgpu_buffer_rsrc_t rs, const unsigned (&off)[2], unsigned sb, unsigned ma, int w, int lane) {
  u32x4 v[2];
  gather<2>(rs, off, sb, v);
  ldswrite16(ma + (w * 80 + lane) * 16, v[0]);
  if (lane < 16) ldswrite16(ma + (w * 80 + 64 + lane) * 16, v[1]);
  BAR();
}

DEV void chain_qk(const Params& p, int h, unsigned lb, int w, int lane) {
  bf16x16 W[20];
  load_weights<0>(p, h, lb, w, lane, W);
  const __amdgpu_buffer_rsrc_t rs = mkrsrc(p.xch);
  const unsigned ma = lb, T = lb + 10240;
  const unsigned off[2] = {mapoff(MEM_OFF + (w * 80 + lane) * 16), mapoff(MEM_OFF + (w * 80 + 64 + (lane & 15)) * 16)};
  const int g = lane >> 4, c16 = lane & 15;
  const int idx = (lane & 8) ? 0xEEEE : 0x4444;
  const unsigned tw = T + (w >> 2) * 2048 + (32 * (w & 3) + 16 * (g >> 1) + c16) * 16 + (g & 1) * 8;
  const unsigned tr = T + (4 * g + (c16 >> 2)) * 16 + (lane & 1) * 8;
  const float sc = 0.08838834764831845f * 1.4426950408889634f;
  for (int s = 0; s < NSEG; ++s) {
    const unsigned sb = slotbase(s);
    bf16x8 KX[2][4];
    if (w == 0) {
#pragma unroll
      for (int tt = 0; tt < 2; ++tt)
#pragma unroll
        for (int kk = 0; kk < 4; ++kk) KX[tt][kk] = *(const bf16x8*)(p.ws + KF_OFF + ((size_t)((s * 32 + h) * 2 + tt) * 4 + kk) * 1024 + lane * 16);
    }
    gather_mem(rs, off, sb, ma, w, lane);
    f32x4 a0 = {}, a1 = {};
#pragma unroll
    for (int kp = 0; kp < 10; ++kp) {
      bf16x8 A = ldsread16(ma + kp * 1024 + lane * 16);
      a0 = smfma(A, W[2 * kp], a0, idx);
      a1 = smfma(A, W[2 * kp + 1], a1, idx);
    }
    float r[4];
#pragma unroll
    for (int i = 0; i < 4; ++i) { float x = a0[i], y = a1[i]; swapadd(x, y, r[i]); }
    ldswrite8(tw, (u32x2){pk2(r[0], r[1]), pk2(r[2], r[3])});
    BAR();
    if (w == 0) {
      f32x4 s0 = {}, s1 = {}, sm = {};
#pragma unroll
      for (int kk = 0; kk < 4; ++kk) {
        bf16x8 qf = cat4(trread(tr + kk * 512), trread(tr + kk * 512 + 256));
        bf16x8 kf = cat4(trread(tr + 2048 + kk * 512), trread(tr + 2048 + kk * 512 + 256));
        s0 = mfma16(KX[0][kk], qf, s0);
        s1 = mfma16(KX[1][kk], qf, s1);
        sm = mfma16(kf, qf, sm);
      }
      if (s < NSEG - 1) {
        float m = fmaxf(fmaxf(fmaxf(s0[0], s0[1]), fmaxf(s0[2], s0[3])), fmaxf(fmaxf(s1[0], s1[1]), fmaxf(s1[2], s1[3])));
        m = fmaxf(m, fmaxf(fmaxf(sm[0], sm[1]), fmaxf(sm[2], sm[3])));
        m = fmaxf(m, xor16(m)); m = fmaxf(m, xor32(m));
        float e0[4], e1[4], em[4], sum = 0.f, summ = 0.f;
        const float mc = -m * sc;
#pragma unroll
        for (int i = 0; i < 4; ++i) {
          e0[i] = __builtin_amdgcn_exp2f(fmaf(s0[i], sc, mc)); e1[i] = __builtin_amdgcn_exp2f(fmaf(s1[i], sc, mc)); em[i] = __builtin_amdgcn_exp2f(fmaf(sm[i], sc, mc));
          sum += e0[i] + e1[i]; summ += em[i];
        }
        sum += 0.5f * summ;
        sum += xor16(sum); sum += xor32(sum);
        const float inv = __builtin_amdgcn_rcpf(sum);
        u32x4 px = {nosent(pk2(e0[0] * inv, e0[1] * inv)), nosent(pk2(e0[2] * inv, e0[3] * inv)), nosent(pk2(e1[0] * inv, e1[1] * inv)), nosent(pk2(e1[2] * inv, e1[3] * inv))};
        u32x2 pm = {nosent(pk2(em[0] * inv, em[1] * inv)), nosent(pk2(em[2] * inv, em[3] * inv))};
        const unsigned pb = P_OFF + (c16 >> 3) * P_B + h * 640;
        bst16(px, rs, mapoff(pb + g * 128 + (c16 & 7) * 16), sb);
        if (g < 2) bst8(pm, rs, mapoff(pb + 512 + (c16 & 7) * 16 + g * 8), sb);
      }
    } else if (w == 1) {
#pragma unroll
      for (int kk = 0; kk < 4; ++kk) {
        bf16x8 kf = cat4(trread(tr + 2048 + kk * 512), trread(tr + 2048 + kk * 512 + 256));
        if (c16 < 8) bst16(__builtin_bit_cast(u32x4, kf), rs, mapoff(KM_OFF + h * 2048 + kk * 512 + g * 128 + c16 * 16), sb);
      }
    }
  }
}

DEV void chain_vo(const Params& p, int h, unsigned lb, int w, int lane) {
  bf16x16 W[20];
  load_weights<1>(p, h, lb, w, lane, W);
  const __amdgpu_buffer_rsrc_t rs = mkrsrc(p.xch);
  const unsigned ma = lb, T = lb + 10240;
  const unsigned off[2] = {mapoff(MEM_OFF + MEM_B + (w * 80 + lane) * 16), mapoff(MEM_OFF + MEM_B + (w * 80 + 64 + (lane & 15)) * 16)};
  const int g = lane >> 4, c16 = lane & 15;
  const int idx = (lane & 8) ? 0xEEEE : 0x4444;
  const unsigned tw = T + (16 * w + c16) * 16 + (g & 1) * 8 + (g >> 1) * 4;
  const int qq = c16 >> 2, pp = lane & 3;
  const unsigned tr = T + (16 * g + (qq >> 1) * 4 + (qq & 1) + 2 * (pp >> 1)) * 16 + (pp & 1) * 8;
  for (int s = 0; s < NSEG; ++s) {
    const unsigned sb = slotbase(s);
    gather_mem(rs, off, sb, ma, w, lane);
    f32x4 a0 = {};
#pragma unroll
    for (int kp = 0; kp < 10; ++kp) {
      bf16x8 A = ldsread16(ma + kp * 1024 + lane * 16);
      a0 = smfma(A, W[kp], a0, idx);
    }
    float r0, r1;
    { float x = a0[0], y = a0[2]; swapadd(x, y, r0); }
    { float x = a0[1], y = a0[3]; swapadd(x, y, r1); }
    *(LAS unsigned*)(uintptr_t)tw = pk2(r0, r1);
    BAR();
    bf16x8 V0 = cat4(trread(tr), trread(tr + 128)), V1 = cat4(trread(tr + 1024), trread(tr + 1024 + 128));
    f32x4 y[5];
#pragma unroll
    for (int c = 0; c < 5; ++c) {
      y[c] = (f32x4){};
      y[c] = smfma(V0, W[10 + 2 * c], y[c], idx);
      y[c] = smfma(V1, W[10 + 2 * c + 1], y[c], idx);
    }
#pragma unroll
    for (int c = 0; c < 5; c += 2) {
      float r[4];
      const int c2 = c + 1 < 5 ? c + 1 : c;
#pragma unroll
      for (int i = 0; i < 4; ++i) { float x = y[c][i], z = y[c2][i]; swapadd(x, z, r[i]); }
      const int cc = (g >> 1) ? c2 : c;
      u32x2 o = {nosent(pk2(r[0], r[1])), nosent(pk2(r[2], r[3]))};
      if (c2 != c || g < 2) bst8(o, rs, mapoff(Y_OFF + ((8 * cc + w) * 32 + h) * 256 + c16 * 16 + (g & 1) * 8), sb);
    }
  }
}

DEV void chain_r(const Params& p, int j, int rep, unsigned lb, int w, int lane) {
  const __amdgpu_buffer_rsrc_t rs = mkrsrc(p.xch);
  const int g = lane >> 4, c16 = lane & 15;
  const unsigned mo = mapoff(MEM_OFF + (g & 1) * MEM_B + (j * 16 + c16) * 16);
  if (w == 0 && lane < 32) {
    const unsigned* src = (const unsigned*)(p.mem0 + (c16 & 7) * DM + j * 16) + (c16 >> 3);
    u32x4 v = {nosent(src[0]), nosent(src[2]), nosent(src[4]), nosent(src[6])};
    bst16(v, rs, mo, slotbase(0));
  }
  unsigned off[4];
  const unsigned pb = P_OFF + rep * P_B;
  off[0] = mapoff(pb + (4 * w + (c16 >> 3)) * 640 + g * 128 + (c16 & 7) * 16);
  off[1] = mapoff(pb + (4 * w + 2 + (c16 >> 3)) * 640 + g * 128 + (c16 & 7) * 16);
  off[2] = mapoff(pb + (4 * w + g) * 640 + 512 + (c16 & 7) * 16);
  off[3] = mapoff(Y_OFF + (j * 32 + 4 * w + g) * 256 + c16 * 16);
  const unsigned red = lb;
  for (int s = 0; s < NSEG - 1; ++s) {
    const unsigned sb = slotbase(s), sb1 = slotbase(s + 1);
    bf16x8 U[4];
#pragma unroll
    for (int i = 0; i < 4; ++i) U[i] = *(const bf16x8*)(p.ws + UF_OFF + ((size_t)(s * 40 + j) * 32 + 4 * w + i) * 1024 + lane * 16);
    u32x4 v[4];
    gather<4>(rs, off, sb, v);
    f32x4 lo = {}, hi = {};
    lo = mfma16(__builtin_bit_cast(bf16x8, v[0]), U[0], lo);
    hi = mfma16(__builtin_bit_cast(bf16x8, v[0]), U[1], hi);
    lo = mfma16(__builtin_bit_cast(bf16x8, v[1]), U[2], lo);
    hi = mfma16(__builtin_bit_cast(bf16x8, v[1]), U[3], hi);
    lo = mfma16(__builtin_bit_cast(bf16x8, v[2]), __builtin_bit_cast(bf16x8, v[3]), lo);
    f32x4 sel = lane < 32 ? lo : hi;
    ldswrite16(red + (s & 1) * 8192 + (w * 64 + lane) * 16, __builtin_bit_cast(u32x4, sel));
    BAR();
    if (w == 0) {
      f32x4 t = {};
#pragma unroll
      for (int i = 0; i < 8; ++i) t += *(LAS f32x4*)(uintptr_t)(red + (s & 1) * 8192 + (i * 64 + lane) * 16);
#pragma unroll
      for (int i = 0; i < 4; ++i) t[i] += xor32(t[i]);
      const unsigned mt = lb + 16384;
      if (lane < 32) {
#pragma unroll
        for (int i = 0; i < 4; ++i)
          *(LAS bf16*)(uintptr_t)(mt + ((((c16 >> 1) & 1) * 8 + 4 * g + i) * 8 + (c16 >> 2) * 2 + (c16 & 1)) * 2) = (bf16)t[i];
      }
      WAIT_LGKM0();
      u32x4 o = *(LAS u32x4*)(uintptr_t)(mt + c16 * 16);
      o.x = nosent(o.x); o.y = nosent(o.y); o.z = nosent(o.z); o.w = nosent(o.w);
      if (lane < 32) bst16(o, rs, mo, sb1);
    }
  }
}

__global__ void __launch_bounds__(512, 2) k_chain(Params p) {
  extern __shared__ __attribute__((aligned(16))) char lds[];
  const unsigned lb = (unsigned)(uintptr_t)(LAS char*)lds;
  const int b = blockIdx.x, lane = threadIdx.x & 63, w = __builtin_amdgcn_readfirstlane(threadIdx.x >> 6);
  const int xg = b & 7, r = b >> 3;
  if (xg == 0) chain_qk(p, r, lb, w, lane);
  else if (xg == 1) chain_vo(p, r, lb, w, lane);
  else if (xg < 4 && r < 20) chain_r(p, (xg - 2) * 20 + r, xg - 2, lb, w, lane);
}

DEV void c1_task(const Params& p, int s, int h, int lane) {
  const int g = lane >> 4, c16 = lane & 15;
  const char* qb = p.ws + QF_OFF + (size_t)(s * 32 + h) * 8192 + lane * 16;
  const char* kb = p.ws + KF_OFF + (size_t)(s * 32 + h) * 8192 + lane * 16;
  bf16x8 Q[2][4], K[2][4], KM[4];
#pragma unroll
  for (int tt = 0; tt < 2; ++tt)
#pragma unroll
    for (int kk = 0; kk < 4; ++kk) { Q[tt][kk] = *(const bf16x8*)(qb + (tt * 4 + kk) * 1024); K[tt][kk] = *(const bf16x8*)(kb + (tt * 4 + kk) * 1024); }
#pragma unroll
  for (int kk = 0; kk < 4; ++kk) KM[kk] = *(const bf16x8*)(p.xch + ((size_t)s << 20) + mapoff(KM_OFF + h * 2048 + kk * 512 + g * 128 + (c16 & 7) * 16));
  const float scale = 0.08838834764831845f, l2e = 1.4426950408889634f;
#pragma unroll
  for (int tt = 0; tt < 2; ++tt) {
    f32x4 s0 = {}, s1 = {}, sm = {};
#pragma unroll
    for (int kk = 0; kk < 4; ++kk) {
      s0 = mfma16(K[0][kk], Q[tt][kk], s0);
      if (tt == 1) s1 = mfma16(K[1][kk], Q[tt][kk], s1);
      sm = mfma16(KM[kk], Q[tt][kk], sm);
    }
    float v0[4], v1[4], vm[4];
    float m = -3.0e38f;
#pragma unroll
    for (int i = 0; i < 4; ++i) {
      const bool dia = (4 * g + i) <= c16;
      v0[i] = bfr(bfr(s0[i]) * scale); v1[i] = bfr(bfr(s1[i]) * scale); vm[i] = bfr(bfr(sm[i]) * scale);
      if (tt == 0) { v0[i] = dia ? v0[i] : -1.0e9f; v1[i] = -1.0e9f; } else v1[i] = dia ? v1[i] : -1.0e9f;
      m = fmaxf(m, fmaxf(v0[i], fmaxf(v1[i], vm[i])));
    }
    m = fmaxf(m, xor16(m)); m = fmaxf(m, xor32(m));
    float sum = 0.f, summ = 0.f;
#pragma unroll
    for (int i = 0; i < 4; ++i) {
      v0[i] = __builtin_amdgcn_exp2f((v0[i] - m) * l2e); v1[i] = __builtin_amdgcn_exp2f((v1[i] - m) * l2e); vm[i] = __builtin_amdgcn_exp2f((vm[i] - m) * l2e);
      sum += v0[i] + v1[i]; summ += vm[i];
    }
    sum += 0.5f * summ;
    sum += xor16(sum); sum += xor32(sum);
    const float inv = 1.0f / sum;
    u32x4 px = {pk2(v0[0] * inv, v0[1] * inv), pk2(v0[2] * inv, v0[3] * inv), pk2(v1[0] * inv, v1[1] * inv), pk2(v1[2] * inv, v1[3] * inv)};
    u32x2 pm = {pk2(vm[0] * inv, vm[1] * inv), pk2(vm[2] * inv, vm[3] * inv)};
    *(u32x4*)(p.ws + PX_OFF + ((size_t)(s * 32 + h) * 2 + tt) * 1024 + lane * 16) = px;
    if (g < 2) *(u32x2*)(p.ws + PM_OFF + ((size_t)(s * 8 + (h >> 2)) * 2 + tt) * 1024 + ((h & 3) * 16 + c16) * 16 + g * 8) = pm;
  }
}
__global__ void __launch_bounds__(512, 2) k_c1(Params p) {
  const int lane = threadIdx.x & 63, w = __builtin_amdgcn_readfirstlane(threadIdx.x >> 6);
  const int t = blockIdx.x * 8 + w;
  c1_task(p, t >> 5, t & 31, lane);
}

DEV void c2_task(const Params& p, int s, int cg, unsigned lb, int w, int lane) {
#pragma unroll
  for (int i = 0; i < 8; ++i) glds16(p.ws + PX_OFF + (size_t)s * 65536 + (w * 8 + i) * 1024 + lane * 16, __builtin_amdgcn_readfirstlane(lb + (w * 8 + i) * 1024));
#pragma unroll
  for (int i = 0; i < 2; ++i) glds16(p.ws + PM_OFF + (size_t)s * 16384 + (w * 2 + i) * 1024 + lane * 16, __builtin_amdgcn_readfirstlane(lb + 65536 + (w * 2 + i) * 1024));
  const int g = lane >> 4, c16 = lane & 15;
  const int j = cg * 8 + w;
  const char* ub = p.ws + UF_OFF + (size_t)(s * 40 + j) * 32768 + lane * 16;
  bf16x8 U[32], Y[8];
#pragma unroll
  for (int h = 0; h < 32; ++h) U[h] = *(const bf16x8*)(ub + h * 1024);
#pragma unroll
  for (int hq = 0; hq < 8; ++hq) Y[hq] = *(const bf16x8*)(p.xch + ((size_t)s << 20) + mapoff(Y_OFF + (j * 32 + 4 * hq + g) * 256 + c16 * 16));
  WAIT_VM(0);
  BAR();
  f32x4 a0 = {}, a1 = {};
#pragma unroll
  for (int h = 0; h < 32; ++h) {
    a0 = mfma16(U[h], ldsread16(lb + (h * 2) * 1024 + lane * 16), a0);
    a1 = mfma16(U[h], ldsread16(lb + (h * 2 + 1) * 1024 + lane * 16), a1);
  }
#pragma unroll
  for (int hq = 0; hq < 8; ++hq) {
    a0 = mfma16(Y[hq], ldsread16(lb + 65536 + (hq * 2) * 1024 + lane * 16), a0);
    a1 = mfma16(Y[hq], ldsread16(lb + 65536 + (hq * 2 + 1) * 1024 + lane * 16), a1);
  }
#pragma unroll
  for (int tt = 0; tt < 2; ++tt) {
    const f32x4 a = tt ? a1 : a0;
    const int t = 32 * s + 16 * tt + c16;
    const float gt = ((const float*)(p.ws + G_OFF))[t];
    const float og = bfr(1.0f - gt);
    const u32x2 bb = *(const u32x2*)(p.ws + BM_OFF + ((size_t)t * DM + 16 * j + 4 * g) * 2);
    const float bv[4] = {bflo(bb.x), bfhi(bb.x), bflo(bb.y), bfhi(bb.y)};
    float o[4];
#pragma unroll
    for (int i = 0; i < 4; ++i) o[i] = bfr(gt * bfr(a[i])) + bfr(og * bv[i]);
    *(u32x2*)((char*)p.out + ((size_t)t * DM + 16 * j + 4 * g) * 2) = (u32x2){pk2(o[0], o[1]), pk2(o[2], o[3])};
  }
}
__global__ void __launch_bounds__(512, 2) k_c2(Params p) {
  extern __shared__ __attribute__((aligned(16))) char lds[];
  const unsigned lb = (unsigned)(uintptr_t)(LAS char*)lds;
  const int lane = threadIdx.x & 63, w = __builtin_amdgcn_readfirstlane(threadIdx.x >> 6);
  c2_task(p, blockIdx.x / 5, blockIdx.x % 5, lb, w, lane);
}

struct cf { float re, im; };
DEV cf csq(cf a) { return {fmaf(a.re, a.re, -a.im * a.im), 2.0f * a.re * a.im}; }
DEV cf cmul(cf a, cf b) { return {fmaf(a.re, b.re, -a.im * b.im), fmaf(a.re, b.im, a.im * b.re)}; }
DEV cf cexp_l(float are, float aim, float l) {
  const float mag = __builtin_amdgcn_exp2f(are * l * 1.4426950408889634f);
  const float th = aim * l;
  const float i2p_hi = 0.15915494309189535f, i2p_lo = 6.4206382e-09f;
  const float hi = th * i2p_hi;
  float lo = fmaf(th, i2p_hi, -hi); lo = fmaf(th, i2p_lo, lo);
  const float fr = __builtin_amdgcn_fractf(hi) + lo;
  return {mag * __builtin_amdgcn_cosf(fr), mag * __builtin_amdgcn_sinf(fr)};
}
constexpr int XPAD = 272;
constexpr int XCOL_B = (SEQ + XPAD) * 2;
constexpr int KR_N = 2112;
constexpr int KR_X0 = 2080;
DEV void ssm_task(const Params& p, int cgp, unsigned lb, int w, int lane, int tid) {
  const int d0 = cgp * 8, d = d0 + w;
  const unsigned xc = lb;
  const unsigned kr = lb + 37120;
  const unsigned kt = kr + 67584;
  for (int t = tid; t < SEQ; t += 512) {
    u32x4 v = *(const u32x4*)(p.x + (size_t)t * DM + d0);
#pragma unroll
    for (int c = 0; c < 4; ++c) {
      *(LAS unsigned short*)(uintptr_t)(xc + (2 * c) * XCOL_B + (XPAD + t) * 2) = (unsigned short)(v[c] & 0xffff);
      *(LAS unsigned short*)(uintptr_t)(xc + (2 * c + 1) * XCOL_B + (XPAD + t) * 2) = (unsigned short)(v[c] >> 16);
    }
  }
  for (int i = lane; i < XPAD / 2; i += 64) *(LAS unsigned*)(uintptr_t)(xc + w * XCOL_B + i * 4) = 0u;
  const int c16 = lane & 15, kq = lane >> 4;
  const float dt = bfr(__expf((float)p.logdt[d]));
  f32x4 T0 = {}, T1 = {}, T2 = {}, T3 = {};
  float k2048 = 0.f;
#pragma unroll 1
  for (int u = 0; u < 32; ++u) {
    const int n = d * 128 + 4 * u + kq;
    const float are = dt * (float)p.Are[n], aim = dt * (float)p.Aim[n];
    const cf dB = {dt * (float)p.Bre[n], dt * (float)p.Bim[n]};
    const cf C = {(float)p.Cre[n], (float)p.Cim[n]};
    const cf c = cmul(C, dB);
    cf Z = cexp_l(are, aim, (float)(16 * c16)), R = cexp_l(are, aim, (float)c16);
    cf L = cmul(c, Z);
    T0 = __builtin_amdgcn_mfma_f32_16x16x4f32(L.re, R.re, T0, 0, 0, 0); T0 = __builtin_amdgcn_mfma_f32_16x16x4f32(-L.im, R.im, T0, 0, 0, 0);
    Z = csq(Z); R = csq(R); L = cmul(c, Z);
    T1 = __builtin_amdgcn_mfma_f32_16x16x4f32(L.re, R.re, T1, 0, 0, 0); T1 = __builtin_amdgcn_mfma_f32_16x16x4f32(-L.im, R.im, T1, 0, 0, 0);
    Z = csq(Z); R = csq(R); L = cmul(c, Z);
    T2 = __builtin_amdgcn_mfma_f32_16x16x4f32(L.re, R.re, T2, 0, 0, 0); T2 = __builtin_amdgcn_mfma_f32_16x16x4f32(-L.im, R.im, T2, 0, 0, 0);
    Z = csq(Z); R = csq(R); L = cmul(c, Z);
    T3 = __builtin_amdgcn_mfma_f32_16x16x4f32(L.re, R.re, T3, 0, 0, 0); T3 = __builtin_amdgcn_mfma_f32_16x16x4f32(-L.im, R.im, T3, 0, 0, 0);
    Z = csq(Z);
    k2048 += fmaf(c.re, Z.re, -c.im * Z.im);
  }
  k2048 += xor16(k2048); k2048 += xor32(k2048);
  const unsigned tb = kt + w * 4112;
#pragma unroll
  for (int i = 0; i < 4; ++i) {
    const unsigned o = ((4 * kq + i) * 16 + c16) * 4;
    *(LAS float*)(uintptr_t)(tb + o) = T0[i]; *(LAS float*)(uintptr_t)(tb + 1024 + o) = T1[i];
    *(LAS float*)(uintptr_t)(tb + 2048 + o) = T2[i]; *(LAS float*)(uintptr_t)(tb + 3072 + o) = T3[i];
  }
  if (lane == 8) *(LAS float*)(uintptr_t)(tb + 4096) = k2048;
  WAIT_LGKM0();
  const unsigned kh = kr + w * (KR_N * 4), kl = kh + KR_N * 2;
  for (int x = lane; x < KR_N; x += 64) {
    const int l = KR_X0 - x;
    float v = 0.f;
    if (l >= 0 && l < SEQ) {
      const int lr = (int)bfr((float)l);
      const int o = lr < 256 ? 0 : lr < 512 ? 1 : lr < 1024 ? 2 : 3;
      if (lr == 2048) v = *(LAS float*)(uintptr_t)(tb + 4096);
      else v = *(LAS float*)(uintptr_t)(tb + o * 1024 + (lr >> o) * 4);
    }
    const bf16 hi = (bf16)v; const bf16 lo = (bf16)(v - (float)hi);
    *(LAS bf16*)(uintptr_t)(kh + x * 2) = hi; *(LAS bf16*)(uintptr_t)(kl + x * 2) = lo;
  }
  BAR();
  f32x4 acc[8] = {};
  const int g = kq;
  const int xe = KR_X0 - 16 + 8 * g - c16;
  const unsigned ash = (xe & 1) * 2;
  const unsigned abase = (unsigned)(xe >> 1) * 4;
  const unsigned bb = xc + w * XCOL_B + (XPAD - 16 + 16 * c16 + 8 * g) * 2;
#pragma unroll
  for (int tq = 0; tq < 8; ++tq) {
#pragma unroll 1
    for (int r = 0; r < 8; ++r) {
      const int m = 16 * tq + 2 * r;
      bf16x8 Ah, Al;
      {
        const LAS unsigned* q = (const LAS unsigned*)(uintptr_t)(kh + abase - m * 32);
        unsigned d0_ = q[0], d1 = q[1], d2 = q[2], d3 = q[3], d4 = q[4];
        u32x4 o = {__builtin_amdgcn_alignbyte(d1, d0_, ash), __builtin_amdgcn_alignbyte(d2, d1, ash), __builtin_amdgcn_alignbyte(d3, d2, ash), __builtin_amdgcn_alignbyte(d4, d3, ash)};
        Ah = __builtin_bit_cast(bf16x8, o);
        const LAS unsigned* q2 = (const LAS unsigned*)(uintptr_t)(kl + abase - m * 32);
        d0_ = q2[0]; d1 = q2[1]; d2 = q2[2]; d3 = q2[3]; d4 = q2[4];
        u32x4 o2 = {__builtin_amdgcn_alignbyte(d1, d0_, ash), __builtin_amdgcn_alignbyte(d2, d1, ash), __builtin_amdgcn_alignbyte(d3, d2, ash), __builtin_amdgcn_alignbyte(d4, d3, ash)};
        Al = __builtin_bit_cast(bf16x8, o2);
      }
#pragma unroll
      for (int tau = tq; tau < 8; ++tau) {
        bf16x8 B = ldsread16(bb + (16 * tau - m) * 32);
        acc[tau] = mfma16(Ah, B, acc[tau]);
        acc[tau] = mfma16(Al, B, acc[tau]);
      }
    }
  }
  const unsigned yb = kt;
#pragma unroll
  for (int tau = 0; tau < 8; ++tau)
#pragma unroll
    for (int i = 0; i < 4; ++i) *(LAS bf16*)(uintptr_t)(yb + (16 * (16 * tau + c16) + 4 * g + i) * 16 + w * 2) = (bf16)acc[tau][i];
  BAR();
  const u32x4 dsk = *(const u32x4*)(p.Dskip + d0);
  for (int t = tid; t < SEQ; t += 512) {
    const u32x4 xv = *(const u32x4*)(p.x + (size_t)t * DM + d0);
    const u32x4 yv = *(LAS u32x4*)(uintptr_t)(yb + t * 16);
    u32x4 o;
#pragma unroll
    for (int c = 0; c < 4; ++c) o[c] = pk2(bflo(yv[c]) + bfr(bflo(xv[c]) * bflo(dsk[c])), bfhi(yv[c]) + bfr(bfhi(xv[c]) * bfhi(dsk[c])));
    *(u32x4*)(p.ws + BM_OFF + ((size_t)t * DM + d0) * 2) = o;
  }
}
DEV void gate_task(const Params& p, int t0, int nt, int w, int lane) {
  for (int t = t0 + w; t < t0 + nt; t += 8) {
    float s = 0.f;
    for (int c = lane; c < 80; c += 64) {
      const u32x4 xv = *(const u32x4*)(p.x + (size_t)t * DM + c * 8), gv = *(const u32x4*)(p.gatew + c * 8);
#pragma unroll
      for (int i = 0; i < 4; ++i) s += bfr(bflo(xv[i]) * bflo(gv[i])) + bfr(bfhi(xv[i]) * bfhi(gv[i]));
    }
#pragma unroll
    for (int o = 1; o < 64; o <<= 1) s += __shfl_xor(s, o);
    s = bfr(s);
    if (lane == 0) ((float*)(p.ws + G_OFF))[t] = bfr(1.0f / (1.0f + __expf(-s)));
  }
}
__global__ void __launch_bounds__(512, 2) k_ssm(Params p) {
  extern __shared__ __attribute__((aligned(16))) char lds[];
  const unsigned lb = (unsigned)(uintptr_t)(LAS char*)lds;
  const int lane = threadIdx.x & 63, w = __builtin_amdgcn_readfirstlane(threadIdx.x >> 6);
  if (blockIdx.x < 80) ssm_task(p, blockIdx.x, lb, w, lane, threadIdx.x);
  else gate_task(p, (blockIdx.x - 80) * 64, 64, w, lane);
}

extern "C" void kernel_launch(void* const* d_in, const int* in_sizes, int n_in, void* d_out, int out_size, void* d_ws, size_t ws_size,
                              hipStream_t stream) {
  Params p;
  p.x = (const bf16*)d_in[0]; p.mem0 = (const bf16*)d_in[1]; p.Wq = (const bf16*)d_in[2]; p.Wk = (const bf16*)d_in[3];
  p.Wv = (const bf16*)d_in[4]; p.Wo = (const bf16*)d_in[5];
  p.Are = (const bf16*)d_in[8]; p.Aim = (const bf16*)d_in[9]; p.Bre = (const bf16*)d_in[10]; p.Bim = (const bf16*)d_in[11];
  p.Cre = (const bf16*)d_in[12]; p.Cim = (const bf16*)d_in[13]; p.logdt = (const bf16*)d_in[14]; p.Dskip = (const bf16*)d_in[15];
  p.gatew = (const bf16*)d_in[16];
  p.out = (bf16*)d_out; p.ws = (char*)d_ws;
  p.xch = (char*)(((uintptr_t)d_ws + ((size_t)2 << 20) - 1) & ~(((uintptr_t)2 << 20) - 1));
  const int LDS = 140 * 1024;
  (void)hipFuncSetAttribute((const void*)k_pre, hipFuncAttributeMaxDynamicSharedMemorySize, LDS);
  (void)hipFuncSetAttribute((const void*)k_chain, hipFuncAttributeMaxDynamicSharedMemorySize, LDS);
  (void)hipFuncSetAttribute((const void*)k_c2, hipFuncAttributeMaxDynamicSharedMemorySize, LDS);
  (void)hipFuncSetAttribute((const void*)k_ssm, hipFuncAttributeMaxDynamicSharedMemorySize, LDS);
  (void)hipMemsetAsync(p.xch, 0xFF, (size_t)64 << 20, stream);
  hipLaunchKernelGGL(k_pre, dim3(256), dim3(512), LDS, stream, p);
  hipLaunchKernelGGL(k_ssm, dim3(112), dim3(512), LDS, stream, p);
  hipLaunchKernelGGL(k_chain, dim3(256), dim3(512), LDS, stream, p);
  hipLaunchKernelGGL(k_c1, dim3(256), dim3(512), 0, stream, p);
  hipLaunchKernelGGL(k_c2, dim3(320), dim3(512), LDS, stream, p);
}
```

```cpp
#include <hip/hip_runtime.h>
#include <hip/hip_bf16.h>
#include <stdint.h>

typedef __bf16 bf16;
typedef bf16 bf16x2 __attribute__((ext_vector_type(2)));
typedef bf16 bf16x4 __attribute__((ext_vector_type(4)));
typedef bf16 bf16x8 __attribute__((ext_vector_type(8)));
typedef bf16 bf16x16 __attribute__((ext_vector_type(16)));
typedef float f32x2 __attribute__((ext_vector_type(2)));
typedef float f32x4 __attribute__((ext_vector_type(4)));
typedef unsigned u32x2 __attribute__((ext_vector_type(2)));
typedef unsigned u32x4 __attribute__((ext_vector_type(4)));
#define LAS __attribute__((address_space(3)))
#define DEV __device__ __forceinline__

constexpr int SEQ = 2048, DM = 640, NSEG = 64;
constexpr unsigned SENT = 0xFFFFFFFFu;

constexpr unsigned BELL_OFF = 10240;
constexpr unsigned CAL_OFF = 507904;
constexpr int PAHEAD = 3;
constexpr unsigned CTL_OFF = 66u << 20;
constexpr unsigned QF_OFF = 67u << 20;
constexpr unsigned KF_OFF = QF_OFF + (16u << 20);
constexpr unsigned UF_OFF = KF_OFF + (16u << 20);
constexpr unsigned PX_OFF = UF_OFF + (80u << 20);
constexpr unsigned PM_OFF = PX_OFF + (4u << 20);
constexpr unsigned BM_OFF = PM_OFF + (1u << 20);
constexpr unsigned G_OFF = BM_OFF + (3u << 20);
constexpr unsigned XF_OFF = G_OFF + (8u << 20);
constexpr unsigned F_KQ = CTL_OFF  , F_U = CTL_OFF + 8192  , F_PROG = CTL_OFF + 16384  , F_S = CTL_OFF + 16896  ,
                   F_RDY = CTL_OFF + 17408  , F_X = CTL_OFF + 17920  , F_PAR = CTL_OFF + 18432  , F_ROT = CTL_OFF + 20480;
constexpr unsigned CTL_BYTES = 20480;

struct Params {
  const bf16 *x, *mem0, *Wq, *Wk, *Wv, *Wo;
  const bf16 *Are, *Aim, *Bre, *Bim, *Cre, *Cim, *logdt, *Dskip, *gatew;
  bf16* out;
  char* xch;
};

DEV unsigned mapoff(unsigned o) { return (o & 8191u) | ((o >> 13) << 14); }
#define SIDE ((unsigned)(par >> 63))
#define memoff(rep, o) ((((unsigned)(o)) & 8191u) | (((2 * (((unsigned)(o)) >> 13) + (unsigned)(rep)) ^ SIDE) << 14))
#define poff(rep, o) ((((unsigned)(o)) & 8191u) | (((4 + 2 * (((unsigned)(o)) >> 13) + (unsigned)(rep)) ^ SIDE) << 14))
#define yoff(j, o) (((unsigned)(o)) | (((16 + (unsigned)(j)) ^ ((j) >= 16 ? 1u : 0u) ^ SIDE) << 14))
DEV unsigned slotbase(int s, unsigned long long par) { return __builtin_amdgcn_readfirstlane(((unsigned)s << 20) + ((unsigned)(par >> (s >> 1)) & 1u) * 8192u); }

DEV f32x4 mfma16(bf16x8 a, bf16x8 b, f32x4 c) { return __builtin_amdgcn_mfma_f32_16x16x32_bf16(a, b, c, 0, 0, 0); }
DEV f32x4 smfma(bf16x8 a, bf16x16 b, f32x4 c, int idx) { return __builtin_amdgcn_smfmac_f32_16x16x64_bf16(a, b, c, idx, 0, 0); }
DEV bf16x8 wlo(bf16x16 w) { return __builtin_shufflevector(w, w, 0, 1, 2, 3, 4, 5, 6, 7); }
DEV bf16x8 whi(bf16x16 w) { return __builtin_shufflevector(w, w, 8, 9, 10, 11, 12, 13, 14, 15); }
DEV unsigned pk2(float a, float b) { f32x2 f = {a, b}; return __builtin_bit_cast(unsigned, __builtin_convertvector(f, bf16x2)); }
DEV float bfr(float a) { return (float)(bf16)a; }
DEV float bflo(unsigned u) { return __builtin_bit_cast(float, u << 16); }
DEV float bfhi(unsigned u) { return __builtin_bit_cast(float, u & 0xffff0000u); }
DEV unsigned nosent(unsigned u) { return min(u, 0xFFFFFFFEu); }
DEV bf16x4 trread(unsigned ldsaddr) { return __builtin_amdgcn_ds_read_tr16_b64_v4bf16((LAS bf16x4*)(uintptr_t)ldsaddr); }
DEV bf16x8 cat4(bf16x4 a, bf16x4 b) { return __builtin_shufflevector(a, b, 0, 1, 2, 3, 4, 5, 6, 7); }
DEV bf16x16 cat8(bf16x8 a, bf16x8 b) { return __builtin_shufflevector(a, b, 0, 1, 2, 3, 4, 5, 6, 7, 8, 9, 10, 11, 12, 13, 14, 15); }
DEV bf16x8 ldsread16(unsigned a) { return *(LAS bf16x8*)(uintptr_t)a; }
DEV void ldswrite16(unsigned a, u32x4 v) { *(LAS u32x4*)(uintptr_t)a = v; }
DEV void ldswrite8(unsigned a, u32x2 v) { *(LAS u32x2*)(uintptr_t)a = v; }
DEV void glds16_nt(const void* gsrc, unsigned lds_dst) {
  unsigned keep;
  asm volatile("s_mov_b32 %0, m0\n\ts_mov_b32 m0, %2\n\ts_nop 0\n\tglobal_load_lds_dwordx4 %1, off nt\n\ts_mov_b32 m0, %0"
               : "=&s"(keep) : "v"(gsrc), "s"(lds_dst) : "memory");
}
#define WAIT_VM(n) asm volatile("s_waitcnt vmcnt(" #n ")" ::: "memory")
#define WAIT_LGKM0() asm volatile("s_waitcnt lgkmcnt(0)" ::: "memory")
#define BAR() do { asm volatile("s_waitcnt lgkmcnt(0)" ::: "memory"); __builtin_amdgcn_s_barrier(); asm volatile("" ::: "memory"); } while (0)
DEV unsigned umax4(u32x4 v) { return max(max(v.x, v.y), max(v.z, v.w)); }
DEV __amdgpu_buffer_rsrc_t mkrsrc(const void* p) { return __builtin_amdgcn_make_buffer_rsrc((void*)p, 0, 0x7fffffff, 0x00020000); }
DEV u32x4 bld16(__amdgpu_buffer_rsrc_t r, unsigned vo, unsigned so) { return __builtin_amdgcn_raw_buffer_load_b128(r, vo, so, 16); }
DEV void bst16(u32x4 v, __amdgpu_buffer_rsrc_t r, unsigned vo, unsigned so) {
  __builtin_amdgcn_raw_buffer_store_b128(v, r, vo, so, 16);
  asm volatile("s_nop 1" :: "v"(v) : "memory");
}
DEV void bst8(u32x2 v, __amdgpu_buffer_rsrc_t r, unsigned vo, unsigned so) { __builtin_amdgcn_raw_buffer_store_b64(v, r, vo, so, 16); }
DEV void bst4(unsigned v, __amdgpu_buffer_rsrc_t r, unsigned vo, unsigned so) { __builtin_amdgcn_raw_buffer_store_b32(v, r, vo, so, 16); }
DEV unsigned bld4(__amdgpu_buffer_rsrc_t r, unsigned vo, unsigned so) { return __builtin_amdgcn_raw_buffer_load_b32(r, vo, so, 16); }
DEV bf16x8 bldf(__amdgpu_buffer_rsrc_t r, unsigned vo, unsigned so) { return __builtin_bit_cast(bf16x8, __builtin_amdgcn_raw_buffer_load_b128(r, vo, so, 16)); }
DEV unsigned uni(unsigned v) { return __builtin_amdgcn_readfirstlane(v); }
#define TP 80
#define TM 153
constexpr int TF = TP + 23;
constexpr int ESTEPS = 14, EM = 5;
constexpr int NFINE = 3;
constexpr int NC2 = 30;
constexpr int NPROBE = 12;
constexpr int UDELAY = 7, KDLY = 12, CDELAY = 3, WSLEEP = 32;
constexpr int SPIN_CAP = 1 << 24;
template <int SLP = WSLEEP>
DEV void wait_flags(__amdgpu_buffer_rsrc_t rs, unsigned vo) {
  for (int spins = 0; spins < SPIN_CAP; ++spins) {
    asm volatile("" ::: "memory");
    unsigned v = bld4(rs, vo, 0);
    if (__builtin_amdgcn_ballot_w64(v == SENT) == 0) break;
    __builtin_amdgcn_s_sleep(SLP);
  }
  asm volatile("" ::: "memory");
}
DEV unsigned long long load_par(__amdgpu_buffer_rsrc_t rs, int lane) {
  unsigned v = SENT;
  for (int spins = 0; spins < SPIN_CAP; ++spins) {
    asm volatile("" ::: "memory");
    v = bld4(rs, F_PAR + (lane < 33 ? lane : 0) * 4, 0);
    if (__builtin_amdgcn_ballot_w64(v == SENT) == 0) break;
    __builtin_amdgcn_s_sleep(2);
  }
  const unsigned long long all = (1ull << 33) - 1, alt = 0x5555555555555555ull & all;
  unsigned long long m = __builtin_amdgcn_ballot_w64((v & 1u) != 0) & all;
  const int d = __builtin_popcountll(m ^ alt);
  m = d <= 12 ? alt : d >= 21 ? (alt ^ all) : m;
  int votes = lane < 32 ? (((m >> lane) & 1) ? (v >> 8) & 15u : (v >> 4) & 15u) : 0;
#pragma unroll
  for (int o = 1; o < 32; o <<= 1) votes += __shfl_xor(votes, o);
  return m | (uni(votes) > 128 ? 1ull << 63 : 0ull);
}
DEV float red16(float v, bool mx) {
  auto r = __builtin_amdgcn_permlane16_swap(__builtin_bit_cast(unsigned, v), __builtin_bit_cast(unsigned, v), false, false);
  float a = __builtin_bit_cast(float, (unsigned)r[0]), b = __builtin_bit_cast(float, (unsigned)r[1]);
  return mx ? fmaxf(a, b) : a + b;
}
DEV float red32(float v, bool mx) {
  auto r = __builtin_amdgcn_permlane32_swap(__builtin_bit_cast(unsigned, v), __builtin_bit_cast(unsigned, v), false, false);
  float a = __builtin_bit_cast(float, (unsigned)r[0]), b = __builtin_bit_cast(float, (unsigned)r[1]);
  return mx ? fmaxf(a, b) : a + b;
}
DEV float ror8(float v) { return __builtin_bit_cast(float, __builtin_amdgcn_mov_dpp(__builtin_bit_cast(unsigned, v), 0x128, 0xf, 0xf, true)); }

template <int ROLE, typename HOOK>
DEV void load_weights(const Params& p, int h, unsigned lb, int w, int lane, bf16x16 (&W)[20], HOOK hook) {
  auto issue = [&](int r) {
#pragma unroll
    for (int i = 0; i < 8; ++i) {
      int q = w * 8 + i;
      int T = r * 4 + (q >> 4);
      int row = 4 * (q & 15) + (lane >> 4);
      int slot = lane & 15;
      int f = (row & 3) | (((row >> 3) & 1) << 2);
      int col = (((slot >> 1) ^ f) << 4) + ((slot & 1) << 3);
      const int pr = T >> 1;
      const bf16* src;
      if (ROLE == 0) src = ((T & 1) ? p.Wk : p.Wq) + (size_t)(64 * pr + row) * 4096 + h * 128 + col;
      else if ((T & 1) == 0) src = p.Wv + (size_t)(64 * pr + row) * 4096 + h * 128 + col;
      else src = p.Wo + (size_t)(h * 128 + 64 * (pr & 1) + row) * 640 + (pr >> 1) * 128 + col;
      glds16_nt(src, __builtin_amdgcn_readfirstlane(lb + (r & 1) * 65536 + q * 1024));
    }
  };
  const int bg = lane >> 4, qq = (lane & 15) >> 2, pp = lane & 3;
  const int fx = qq | ((bg & 1) << 2);
  const unsigned tb = (w >> 2) * 16384 + (8 * bg + qq) * 256 + pp * 8;
  const unsigned a0 = tb + (((2 * (w & 3)) ^ fx) << 5), a1 = tb + (((2 * (w & 3) + 1) ^ fx) << 5);
  issue(0); issue(1);
#pragma unroll
  for (int r = 0; r < 5; ++r) {
    if (r < 4) WAIT_VM(8); else WAIT_VM(0);
    BAR();
#pragma unroll
    for (int pr = 0; pr < 2; ++pr) {
      unsigned b0 = lb + (r & 1) * 65536 + pr * 32768 + a0, b1 = lb + (r & 1) * 65536 + pr * 32768 + a1;
      W[(r * 2 + pr) * 2 + 0] = cat8(cat4(trread(b0), trread(b0 + 1024)), cat4(trread(b0 + 8192), trread(b0 + 9216)));
      W[(r * 2 + pr) * 2 + 1] = cat8(cat4(trread(b1), trread(b1 + 1024)), cat4(trread(b1 + 8192), trread(b1 + 9216)));
    }
    BAR();
    if (r + 2 < 5) issue(r + 2);
    if (r == 3) hook();
  }
}

DEV void glds16s(const char* sbase, unsigned voff, unsigned lds_dst) {
  unsigned keep;
  asm volatile("s_mov_b32 %0, m0\n\ts_mov_b32 m0, %2\n\ts_nop 2\n\tglobal_load_lds_dwordx4 %1, %3 sc1\n\ts_mov_b32 m0, %0"
               : "=&s"(keep) : "v"(voff), "s"(lds_dst), "s"(sbase) : "memory");
}
DEV void stage_x1(const Params& p, int s, unsigned dst, int w, int lane, int i) {
  glds16s(p.xch + XF_OFF + (size_t)s * 40960 + (w * 5 + i) * 1024, lane * 16, dst + (w * 5 + i) * 1024);
}
DEV void stage_x(const Params& p, int s, unsigned dst, int w, int lane) {
#pragma unroll
  for (int i = 0; i < 5; ++i) stage_x1(p, s, dst, w, lane, i);
}

#define SB() __builtin_amdgcn_sched_barrier(0)
template <typename MID>
DEV void gemm_x(unsigned xb, const bf16x16 (&W)[20], f32x4 (&acc)[2][2], MID mid) {
  bf16x8 X0[20], X1[20];
#pragma unroll
  for (int kb = 0; kb < 3; ++kb) { X0[kb] = ldsread16(xb + kb * 1024); X1[kb] = ldsread16(xb + (20 + kb) * 1024); }
  SB();
#pragma unroll
  for (int kb = 0; kb < 20; ++kb) {
    const bf16x8 w0 = (kb & 1) ? whi(W[(kb >> 1) * 2]) : wlo(W[(kb >> 1) * 2]), w1 = (kb & 1) ? whi(W[(kb >> 1) * 2 + 1]) : wlo(W[(kb >> 1) * 2 + 1]);
    acc[0][0] = mfma16(w0, X0[kb], acc[0][0]);
    if (kb + 3 < 20) X0[kb + 3] = ldsread16(xb + (kb + 3) * 1024);
    acc[1][0] = mfma16(w1, X0[kb], acc[1][0]);
    SB();
    acc[0][1] = mfma16(w0, X1[kb], acc[0][1]);
    if (kb + 3 < 20) X1[kb + 3] = ldsread16(xb + (20 + kb + 3) * 1024);
    acc[1][1] = mfma16(w1, X1[kb], acc[1][1]);
    SB();
    mid(kb); SB();
  }
}
DEV void worker_qk(const Params& p, int h, unsigned lb, int w, int lane) {
  bf16x16 W[20];
  const __amdgpu_buffer_rsrc_t rs = mkrsrc(p.xch);
  load_weights<0>(p, h, lb, w, lane, W, [&]() { wait_flags(rs, F_X + lane * 4); stage_x(p, 0, lb + 65536, w, lane); });
  const unsigned dsto = ((w >> 2) ? KF_OFF : QF_OFF) + h * 8192 + (w & 3) * 1024 + lane * 16;
  for (int s = 0; s < NSEG; ++s) {
    BAR();
    f32x4 acc[2][2] = {};
    gemm_x(lb + ((s & 1) ^ 1) * 65536 + lane * 16, W, acc, [&](int kb) {
      if (kb < 5) stage_x1(p, min(s + 1, NSEG - 1), lb + (s & 1) * 65536, w, lane, kb);
      if (kb == 7) {
        WAIT_VM(5);
        __builtin_amdgcn_s_barrier();
        if (s > 1 && threadIdx.x == 0) bst4(1u, rs, 0, uni(F_KQ + (h * 64 + s - 1) * 4));
      }
    });
#pragma unroll
    for (int tt = 0; tt < 2; ++tt) {
      u32x4 o = {pk2(acc[0][tt][0], acc[0][tt][1]), pk2(acc[0][tt][2], acc[0][tt][3]), pk2(acc[1][tt][0], acc[1][tt][1]), pk2(acc[1][tt][2], acc[1][tt][3])};
      bst16(o, rs, dsto, uni(s * 262144 + tt * 4096));
    }
    if (s == 0) {
      WAIT_VM(0); BAR();
      if (threadIdx.x == 0) bst4(1u, rs, 0, uni(F_KQ + (h * 64 + s) * 4));
    } else WAIT_VM(2);
  }
  WAIT_VM(0); BAR();
  if (threadIdx.x == 0) bst4(1u, rs, 0, uni(F_KQ + (h * 64 + NSEG - 1) * 4));
}

DEV void worker_vo(const Params& p, int h, unsigned lb, int w, int lane) {
  constexpr int NIT = NSEG;
  bf16x16 W[20];
  const __amdgpu_buffer_rsrc_t rs = mkrsrc(p.xch);
  load_weights<1>(p, h, lb, w, lane, W, [&]() { wait_flags(rs, F_X + lane * 4); stage_x(p, 0, lb + 65536, w, lane); });
  const unsigned vf = lb + 106496;
  const unsigned cnt = lb + 122880;
  if (threadIdx.x < 2) *(LAS unsigned*)(uintptr_t)(cnt + threadIdx.x * 4) = 0u;
  const int g = lane >> 4;
  if (w < 4) {
    const unsigned vw = vf + w * 1024 + ((lane & 15) + 16 * (g >> 1)) * 16 + (g & 1) * 8;
    for (int it = 0; it <= NIT; ++it) {
      BAR();
      if (it < NIT) {
        f32x4 acc[2][2] = {};
        gemm_x(lb + ((it & 1) ^ 1) * 65536 + lane * 16, W, acc, [&](int kb) {
          if (kb < 10) stage_x1(p, min(it + 1, NIT - 1), lb + (it & 1) * 65536, 2 * w, lane, kb);
        });
#pragma unroll
        for (int ct = 0; ct < 2; ++ct)
#pragma unroll
          for (int tt = 0; tt < 2; ++tt)
            ldswrite8(vw + (it & 1) * 8192 + tt * 4096 + ct * 512, (u32x2){pk2(acc[ct][tt][0], acc[ct][tt][1]), pk2(acc[ct][tt][2], acc[ct][tt][3])});
        WAIT_VM(0);
      }
    }
  } else {
    const unsigned dsto = UF_OFF + (2 * (w & 3) * 32 + h) * 1024 + lane * 16;
    auto publish = [&](int s) {
      if (lane == 0) {
        const unsigned old = __hip_atomic_fetch_add((LAS unsigned*)(uintptr_t)(cnt + (s & 1) * 4), 1u, __ATOMIC_RELAXED, __HIP_MEMORY_SCOPE_WORKGROUP);
        if (old == 4u * (s >> 1) + 3u) bst4(1u, rs, 0, uni(F_U + (s * 32 + h) * 4));
      }
    };
    for (int it = 0; it <= NIT; ++it) {
      BAR();
      if (it > 0) {
        const int s = it - 1;
        bf16x8 V[2][4];
#pragma unroll
        for (int tt = 0; tt < 2; ++tt)
#pragma unroll
          for (int kk = 0; kk < 4; ++kk) V[tt][kk] = ldsread16(vf + (s & 1) * 8192 + (tt * 4 + kk) * 1024 + lane * 16);
#pragma unroll
        for (int c = 0; c < 5; ++c) {
          f32x4 u[2][2] = {};
#pragma unroll
          for (int kk = 0; kk < 4; ++kk)
#pragma unroll
            for (int ct = 0; ct < 2; ++ct) {
              const bf16x16 wf = W[(2 * c + (kk >> 1)) * 2 + ct];
              const bf16x8 wb = (kk & 1) ? whi(wf) : wlo(wf);
              u[ct][0] = mfma16(V[0][kk], wb, u[ct][0]);
              u[ct][1] = mfma16(V[1][kk], wb, u[ct][1]);
            }
          if (c == 1 && s > 1) { WAIT_VM(2); publish(s - 1); }
#pragma unroll
          for (int ct = 0; ct < 2; ++ct) {
            u32x4 o = {pk2(u[ct][0][0], u[ct][0][1]), pk2(u[ct][0][2], u[ct][0][3]), pk2(u[ct][1][0], u[ct][1][1]), pk2(u[ct][1][2], u[ct][1][3])};
            bst16(o, rs, dsto, uni((s * 40 + 8 * c + ct) * 32768));
          }
        }
        if (s == 0 || s == NIT - 1) { WAIT_VM(0); publish(s); }
      }
    }
  }
  BAR();
}

DEV void nopwait(int n) {
#pragma unroll 1
  for (int i = 0; i < n; ++i) asm volatile("");
}
DEV unsigned rclock() { unsigned long long t; asm volatile("s_memrealtime %0\n\ts_waitcnt lgkmcnt(0)" : "=s"(t) :: "memory"); return (unsigned)t; }
DEV void pace(__amdgpu_buffer_rsrc_t rs, unsigned so, int ticks) {
  unsigned v = SENT;
  for (int spins = 0; spins < SPIN_CAP && v == SENT; ++spins) {
    asm volatile("" ::: "memory");
    v = uni(bld4(rs, 0, so));
  }
  const unsigned target = v + (unsigned)ticks;
  for (int i = 0; i < 64; ++i) {
    const int rem = (int)(target - rclock());
    if (rem <= 10) { nopwait((max(rem, 0) * 45) >> 6); break; }
    if (rem > 500) break;
  }
}
DEV void gather_mem(__amdgpu_buffer_rsrc_t rs, const unsigned (&off)[3], unsigned sb, unsigned ma, int gw, int lane) {
  u32x4 v[3];
  for (int spins = 0; spins < SPIN_CAP; ++spins) {
    asm volatile("" ::: "memory");
#pragma unroll
    for (int i = 0; i < 3; ++i) v[i] = bld16(rs, off[i], sb);
    if (__builtin_amdgcn_ballot_w64(max(max(umax4(v[0]), umax4(v[1])), umax4(v[2])) == SENT) == 0) break;
  }
  const unsigned d = ma + (gw * 160 + lane) * 16;
  ldswrite16(d, v[0]); ldswrite16(d + 1024, v[1]);
  if (lane < 32) ldswrite16(d + 2048, v[2]);
}
DEV int ready_upto(unsigned long long notset, int shift) { return notset ? (__builtin_ctzll(notset) >> shift) : (64 >> shift); }

template <bool B> struct BoolC { static constexpr bool value = B; };
DEV float max3f(float a, float b, float c) { float r; asm("v_max3_f32 %0, %1, %2, %3" : "=v"(r) : "v"(a), "v"(b), "v"(c)); return r; }
DEV float maxf_(float a, float b) { float r; asm("v_max_f32 %0, %1, %2" : "=v"(r) : "v"(a), "v"(b)); return r; }
DEV float rmax16(float v) {
  auto r = __builtin_amdgcn_permlane16_swap(__builtin_bit_cast(unsigned, v), __builtin_bit_cast(unsigned, v), false, false);
  return maxf_(__builtin_bit_cast(float, (unsigned)r[0]), __builtin_bit_cast(float, (unsigned)r[1]));
}
DEV float rmax32(float v) {
  auto r = __builtin_amdgcn_permlane32_swap(__builtin_bit_cast(unsigned, v), __builtin_bit_cast(unsigned, v), false, false);
  return maxf_(__builtin_bit_cast(float, (unsigned)r[0]), __builtin_bit_cast(float, (unsigned)r[1]));
}

DEV u32x2 project(unsigned ab, const bf16x16 (&W)[20], int idx) {
  bf16x8 A[10];
  f32x4 a0 = {}, a1 = {};
#pragma unroll
  for (int kp = 0; kp < 5; ++kp) A[kp] = ldsread16(ab + kp * 1024);
  SB();
#pragma unroll
  for (int kp = 0; kp < 10; ++kp) {
    a0 = smfma(A[kp], W[2 * kp], a0, idx);
    if (kp + 5 < 10) A[kp + 5] = ldsread16(ab + (kp + 5) * 1024);
    a1 = smfma(A[kp], W[2 * kp + 1], a1, idx);
    SB();
  }
  return (u32x2){pk2(a0[0] + a0[2], a0[1] + a0[3]), pk2(a1[0] + a1[2], a1[1] + a1[3])};
}
DEV void ldswrite4(unsigned a, unsigned v) { *(LAS unsigned*)(uintptr_t)a = v; }

DEV void chain_qk(const Params& p, int h, int rep, unsigned lb, int w, int lane) {
  bf16x16 W[20];
  const __amdgpu_buffer_rsrc_t rs = mkrsrc(p.xch);
  for (int i = 0; i < CDELAY; ++i) __builtin_amdgcn_s_sleep(16);
  load_weights<0>(p, h, lb, w, lane, W, []() {});
  const unsigned long long par = load_par(rs, lane);
  const unsigned ma = lb, Tq = lb + 10240, Tk = lb + 12288, kxb = lb + 16384;
  const unsigned mb = (w & 3) * 2560;
  const unsigned bm = uni(memoff(rep, BELL_OFF + 128));
  const unsigned off[3] = {memoff(rep, mb + lane * 16), memoff(rep, mb + 1024 + lane * 16), memoff(rep, mb + 2048 + (lane & 31) * 16)};
  const int g = lane >> 4, c16 = lane & 15;
  const int idx = (lane & 2) ? 0xEEEE : 0x4444;
  const int we = 32 * (w & 3) + c16;
  const unsigned tw = (w < 4) ? Tq + we * 16 + g * 4 : Tk + we * 32 + g * 8;
  const unsigned tw2 = tw + ((w < 4) ? 256 : 512);
  const unsigned trq = Tq + (4 * g + (c16 >> 2)) * 16 + (lane & 1) * 8;
  const unsigned trk = Tk + (4 * g + (c16 >> 2)) * 32 + (lane & 3) * 8;
  const float sc = 0.08838834764831845f * 1.4426950408889634f;
  const bool hi8 = (lane & 8) != 0;
  unsigned o0, o1, o2;
  o0 = poff(0, h * 640 + g * 128 + (c16 & 7) * 16 + (c16 >> 3) * 8);
  o1 = poff(1, h * 640 + g * 128 + (c16 & 7) * 16 + (c16 >> 3) * 8);
  o2 = poff(c16 >> 3, h * 640 + 512 + (c16 & 7) * 16 + g * 4);
  if (w == 1 || w == 2) { o0 = PX_OFF + (h * 2 + w - 1) * 1024 + lane * 16; o1 = PM_OFF + ((h >> 2) * 2 + w - 1) * 1024 + ((h & 3) * 16 + c16) * 16 + g * 4; }
  if (w == 3) o1 = F_KQ + (h * 64 + lane) * 4;
  int known = 0;
  int fetched = 0;
  auto prefetch_k = [&](int need, int want) {
    for (int spins = 0; known < want && spins < SPIN_CAP; ++spins) {
      asm volatile("" ::: "memory");
      unsigned v = bld4(rs, o1, 0);
      known = ready_upto(__builtin_amdgcn_ballot_w64(v == SENT), 0);
      if (known >= need) break;
      __builtin_amdgcn_s_sleep(4);
    }
    asm volatile("" ::: "memory");
    for (const int upto = max(need, min(known, want)); fetched < upto; ++fetched) {
      const char* src = p.xch + (size_t)(uni(fetched) * 32 + h) * 8192;
      const unsigned d = uni(kxb + (fetched % 3) * 16384);
#pragma unroll
      for (int i = 0; i < 8; ++i) glds16s(src + KF_OFF + i * 1024, lane * 16, d + i * 1024);
#pragma unroll
      for (int i = 0; i < 8; ++i) glds16s(src + QF_OFF + i * 1024, lane * 16, d + 8192 + i * 1024);
    }
  };
  int young = 0;
  if (w == 0) {
    for (int sp = 0; sp < PAHEAD; ++sp) {
      const unsigned sbp = slotbase(sp, par);
      bst8((u32x2){SENT, SENT}, rs, o0, sbp); bst8((u32x2){SENT, SENT}, rs, o1, sbp); bst4(SENT, rs, o2, sbp);
    }
    WAIT_VM(0);
    if (lane == 0) bst4(1u, rs, 0, uni(F_RDY + h * 4));
  }
  if (w == 3) prefetch_k(1, 1);
  if (w >= 4) wait_flags(rs, F_RDY + (64 + min(lane, 39)) * 4);
  auto run = [&](auto is0c) {
  constexpr bool IS0 = decltype(is0c)::value;
  for (int s = 0; s < NSEG; ++s) {
    const unsigned sb = slotbase(s, par);
    if (!IS0 && w >= 4) {
      if (s > 0) pace(rs, slotbase(s - 1, par) + bm, 0);
      gather_mem(rs, off, sb, ma, w & 3, lane);
    }
    else if (!IS0 && w == 3 && young) WAIT_VM(16);
    else if (IS0) { if (s > 0 && s + PAHEAD < NSEG) WAIT_VM(6); else WAIT_VM(0); }
    else if (w == 3) WAIT_VM(0);
    BAR();
    const u32x2 r = project(ma + lane * 16, W, idx);
    ldswrite4(tw, r.x); ldswrite4(tw2, r.y);
    if constexpr (IS0) {
      asm volatile("" ::: "memory"); SB();
      bf16x8 KX[8];
#pragma unroll
      for (int i = 0; i < 8; ++i) KX[i] = ldsread16(kxb + (s % 3) * 16384 + i * 1024 + lane * 16);
      SB();
      asm volatile("s_waitcnt lgkmcnt(8)" ::: "memory");
      __builtin_amdgcn_s_barrier();
      asm volatile("" ::: "memory");
      if (s < NSEG - 1) {
        bf16x8 qf[4], kf[4];
#pragma unroll
        for (int kk = 0; kk < 4; ++kk) {
          qf[kk] = cat4(trread(trq + kk * 512), trread(trq + kk * 512 + 256));
          kf[kk] = cat4(trread(trk + kk * 1024), trread(trk + kk * 1024 + 512));
        }
        SB();
        f32x4 s0 = {}, s1 = {}, sm = {};
#pragma unroll
        for (int kk = 0; kk < 4; ++kk) {
          sm = mfma16(kf[kk], qf[kk], sm);
          s0 = mfma16(KX[kk], qf[kk], s0);
          s1 = mfma16(KX[4 + kk], qf[kk], s1);
        }
        float x[5];
        x[4] = hi8 ? sm[1] : sm[0];
#pragma unroll
        for (int i = 0; i < 4; ++i) x[i] = hi8 ? s1[i] : s0[i];
        float m = max3f(max3f(x[4], x[0], x[1]), x[2], x[3]);
        m = maxf_(m, ror8(m)); m = rmax16(m); m = rmax32(m);
        const float mc = -m * sc;
#pragma unroll
        for (int i = 0; i < 5; ++i) x[i] = __builtin_amdgcn_exp2f(fmaf(x[i], sc, mc));
        float sum = ((x[0] + x[1]) + (x[2] + x[3])) + x[4];
        sum += ror8(sum); sum = red16(sum, false); sum = red32(sum, false);
        const float inv = __builtin_amdgcn_rcpf(sum);
        const u32x2 px = {nosent(pk2(x[0] * inv, x[1] * inv)), nosent(pk2(x[2] * inv, x[3] * inv))};
        bst8(px, rs, o0, sb);
        bst8(px, rs, o1, sb);
        const float pm = x[4] * inv, po = ror8(pm);
        bst4(nosent(pk2(hi8 ? po : pm, hi8 ? pm : po)), rs, o2, sb);
        if (s + PAHEAD < NSEG - 1) {
          const unsigned sbp = slotbase(s + PAHEAD, par);
          bst8((u32x2){SENT, SENT}, rs, o0, sbp); bst8((u32x2){SENT, SENT}, rs, o1, sbp); bst4(SENT, rs, o2, sbp);
        }
      }
    } else {
      if (w == 1 || w == 2) WAIT_VM(0);
      BAR();
      if (w == 1 || w == 2) {
        const bool t1 = w == 2;
        const unsigned fb = kxb + (s % 3) * 16384 + lane * 16;
        f32x4 s0 = {}, s1 = {}, sm = {};
#pragma unroll
        for (int kk = 0; kk < 4; ++kk) {
          const bf16x8 Q = ldsread16(fb + 8192 + (w - 1) * 4096 + kk * 1024);
          s0 = mfma16(ldsread16(fb + kk * 1024), Q, s0);
          s1 = mfma16(ldsread16(fb + 4096 + kk * 1024), Q, s1);
          sm = mfma16(cat4(trread(trk + kk * 1024), trread(trk + kk * 1024 + 512)), Q, sm);
        }
        const float scale = 0.08838834764831845f, l2e = 1.4426950408889634f;
        float v0[4], v1[4], vm[2];
        vm[0] = bfr(bfr(sm[0]) * scale); vm[1] = bfr(bfr(sm[1]) * scale);
        float m = fmaxf(vm[0], vm[1]);
#pragma unroll
        for (int i = 0; i < 4; ++i) {
          const bool dia = (4 * g + i) <= c16;
          v0[i] = (t1 || dia) ? bfr(bfr(s0[i]) * scale) : -1.0e9f;
          v1[i] = (t1 && dia) ? bfr(bfr(s1[i]) * scale) : -1.0e9f;
          m = fmaxf(m, fmaxf(v0[i], v1[i]));
        }
        m = red16(m, true); m = red32(m, true);
        vm[0] = __builtin_amdgcn_exp2f((vm[0] - m) * l2e); vm[1] = __builtin_amdgcn_exp2f((vm[1] - m) * l2e);
        float sum = vm[0] + vm[1];
#pragma unroll
        for (int i = 0; i < 4; ++i) {
          v0[i] = __builtin_amdgcn_exp2f((v0[i] - m) * l2e); v1[i] = __builtin_amdgcn_exp2f((v1[i] - m) * l2e);
          sum += v0[i] + v1[i];
        }
        sum = red16(sum, false); sum = red32(sum, false);
        const float inv = 1.0f / sum;
        bst16((u32x4){nosent(pk2(v0[0] * inv, v0[1] * inv)), nosent(pk2(v0[2] * inv, v0[3] * inv)), nosent(pk2(v1[0] * inv, v1[1] * inv)), nosent(pk2(v1[2] * inv, v1[3] * inv))}, rs, o0, uni(s * 65536));
        bst4(nosent(pk2(vm[0] * inv, vm[1] * inv)), rs, o1, uni(s * 16384));
        if (s + 3 >= NSEG - NFINE && s + 3 < NSEG) { bst16((u32x4){SENT, SENT, SENT, SENT}, rs, o0, uni((s + 3) * 65536)); bst4(SENT, rs, o1, uni((s + 3) * 16384)); }
      } else if (w == 3) {
        __builtin_amdgcn_s_sleep(KDLY);
        prefetch_k(min(s + 2, NSEG), min(s + 3, NSEG));
        young = fetched == s + 3;
      } else if (w == 4) __builtin_amdgcn_s_sleep(10);
    }
  }
  };
  if (w == 0) run(BoolC<true>{}); else run(BoolC<false>{});
}

DEV void chain_vo(const Params& p, int h, int rep, unsigned lb, int w, int lane) {
  bf16x16 W[20];
  const __amdgpu_buffer_rsrc_t rs = mkrsrc(p.xch);
  for (int i = 0; i < CDELAY; ++i) __builtin_amdgcn_s_sleep(16);
  load_weights<1>(p, h, lb, w, lane, W, []() {});
  const unsigned long long par = load_par(rs, lane);
  const unsigned ma = lb, T = lb + 10240;
  const unsigned mb = (w & 3) * 2560;
  const unsigned bm = uni(memoff(rep, BELL_OFF + 128));
  const unsigned off[3] = {memoff(rep, mb + lane * 16), memoff(rep, mb + 1024 + lane * 16), memoff(rep, mb + 2048 + (lane & 31) * 16)};
  const int g = lane >> 4, c16 = lane & 15;
  const int idx = (lane & 2) ? 0xEEEE : 0x4444;
  if (w < 4) {
    const unsigned tw = T + (16 * w + (c16 >> 2) * 2 + (c16 & 1)) * 32 + g * 8 + ((c16 >> 1) & 1) * 4;
    wait_flags(rs, F_RDY + (64 + min(lane, 39)) * 4);
    for (int s = 0; s < NSEG; ++s) {
      const unsigned sb = slotbase(s, par);
      if (s > 0) pace(rs, slotbase(s - 1, par) + bm, 0);
      gather_mem(rs, off, sb, ma, w, lane);
      BAR();
      const u32x2 r = project(ma + lane * 16, W, idx);
      ldswrite4(tw, r.x); ldswrite4(tw + 256, r.y);
      BAR();
      __builtin_amdgcn_s_sleep(8);
    }
  } else {
    const unsigned tr = T + (8 * g + (c16 >> 2)) * 32 + (lane & 3) * 8;
    const unsigned yo = yoff(8 * (g >> 1) + 2 * (w & 3) + (g & 1), h * 256 + c16 * 16), yo2 = yo ^ 16384u;
    const bool ringer = h == 0 && w == 5;
    const unsigned bo = memoff(lane & 1, BELL_OFF + ((lane >> 1) & 1) * 128);
    auto poison = [&](int sp) {
      if (sp < NSEG) {
        const unsigned sbp = slotbase(sp, par);
        if (ringer && lane < 4 && sp >= PAHEAD) bst4(SENT, rs, bo, sbp);
#pragma unroll
        for (int c = 0; c < 3; ++c) if (c < 2 || g < 2) bst16((u32x4){SENT, SENT, SENT, SENT}, rs, c ? yo2 : yo, sbp + c * 262144);
      }
    };
    for (int sp = 0; sp < PAHEAD; ++sp) poison(sp);
    for (int s = 0; s < NSEG; ++s) {
      const unsigned sb = slotbase(s, par);
      WAIT_VM(0);
      BAR();
      if (ringer) { const unsigned t = rclock(); if (lane < 4) bst4(nosent(t + (unsigned)(lane < 2 ? TP : TM + (s < ESTEPS ? EM : 0))), rs, bo, sb); }
      if (s == 0 && threadIdx.x == 256) bst4(1u, rs, 0, uni(F_RDY + (32 + h) * 4));
      if (h == 1 && threadIdx.x == 320) bst4(1u, rs, 0, uni(F_PROG + s * 4));
      if (s > 0) poison(s - 1 + PAHEAD);
      f32x4 y[10];
#pragma unroll
      for (int i = 0; i < 10; ++i) { y[i] = (f32x4){}; asm volatile("" : "+v"(y[i])); }
      BAR();
      const bf16x8 V0 = cat4(trread(tr), trread(tr + 128)), V1 = cat4(trread(tr + 1024), trread(tr + 1024 + 128));
      auto fin = [&](int t) { return nosent(pk2(y[t][0] + y[t][2], y[t][1] + y[t][3])); };
      auto st4 = [&](int q) {
        const unsigned d0 = fin(4 * q), d1 = fin(4 * q + 1), d2 = q < 2 ? fin(4 * q + 2) : d0, d3 = q < 2 ? fin(4 * q + 3) : d1;
        auto a = __builtin_amdgcn_permlane16_swap(d0, d1, false, false);
        auto b = __builtin_amdgcn_permlane16_swap(d2, d3, false, false);
        auto c = __builtin_amdgcn_permlane32_swap((unsigned)a[0], (unsigned)b[0], false, false);
        auto d = __builtin_amdgcn_permlane32_swap((unsigned)a[1], (unsigned)b[1], false, false);
        const u32x4 o = {(unsigned)c[0], (unsigned)d[0], (unsigned)c[1], (unsigned)d[1]};
        if (q < 2 || g < 2) bst16(o, rs, q ? yo2 : yo, sb + q * 262144);
      };
      SB();
#pragma unroll
      for (int c = 0; c < 5; ++c) {
#pragma unroll
        for (int i = 0; i < 4; ++i) y[2 * c + (i & 1)] = smfma((i & 2) ? V1 : V0, W[4 * c + i], y[2 * c + (i & 1)], idx);
        SB();
        if (c == 2) { st4(0); SB(); }
      }
      st4(1);
      SB();
      st4(2);
    }
  }
}

DEV void chain_r(const Params& p, int j, int rep, unsigned lb, int w, int lane) {
  const __amdgpu_buffer_rsrc_t rs = mkrsrc(p.xch);
  if (j < 33) {
    const unsigned q = uni(((unsigned)j << 21) + mapoff(CAL_OFF) + w * 1024);
    u32x4 best = {0xffffffffu, 0xffffffffu, 0xffffffffu, 0xffffffffu};
    for (int it = 0; it < NPROBE / 4; ++it) {
#pragma unroll
      for (int c = 0; c < 4; ++c) {
        unsigned long long t0, t1;
        asm volatile("s_memtime %0\n\ts_waitcnt lgkmcnt(0)" : "=s"(t0) :: "memory");
        if (lane < 32) bst4(it, rs, lane * 4, q + c * 8192);
        WAIT_VM(0);
        asm volatile("s_memtime %0\n\ts_waitcnt lgkmcnt(0)" : "=s"(t1) :: "memory");
        best[c] = min(best[c], (unsigned)(t1 - t0));
      }
    }
    if (lane == 0) ldswrite16(lb + w * 16, best);
    BAR();
    if (threadIdx.x == 0) {
      int vo = 0, ve2 = 0, vo2 = 0;
      for (int i = 0; i < 8; ++i) {
        const u32x4 a = *(LAS u32x4*)(uintptr_t)(lb + i * 16);
        vo += min(a.y, a.w) < min(a.x, a.z); ve2 += a.x < a.z; vo2 += a.y < a.w;
      }
      const unsigned xcc = (unsigned)__builtin_amdgcn_s_getreg(20 | (3 << 11)) & 7u;
      const unsigned se = (xcc & 1u) ? 8 - ve2 : ve2, so = (xcc & 1u) ? 8 - vo2 : vo2;
      bst4(((vo > 4 ? 1u : 0u) ^ (xcc >> 2)) | (se << 4) | (so << 8), rs, 0, uni(F_PAR + j * 4));
    }
  }
  const unsigned long long par = load_par(rs, lane);
  const int g = lane >> 4, c16 = lane & 15, w3 = w & 3;
  const int pos = 4 * ((c16 >> 2) & 1) + (c16 & 3);
  const int pc = 4 * (pos >> 1) + 2 * (c16 >> 3) + (pos & 1);
  const unsigned bq = uni(memoff(rep, BELL_OFF));
  const unsigned red = lb;
  if (w < 4) {
    const unsigned mo = memoff(c16 >> 3, (j * 16 + 4 * ((c16 & 7) >> 1) + 2 * (g >> 1) + (c16 & 1)) * 16 + (g & 1) * 8);
    if (w == 0) {
      const unsigned* src = (const unsigned*)(p.mem0 + (c16 & 7) * DM + j * 16) + (g >> 1) + (g & 1) * 4;
      bst8((u32x2){nosent(src[0]), nosent(src[2])}, rs, mo, slotbase(0, par));
      for (int sp = 1; sp < PAHEAD; ++sp) bst8((u32x2){SENT, SENT}, rs, mo, slotbase(sp, par));
      if (j == 0 && lane < 4)
        for (int sp = 0; sp < PAHEAD; ++sp) bst4(SENT, rs, memoff(lane & 1, BELL_OFF + ((lane >> 1) & 1) * 128), slotbase(sp, par));
      WAIT_VM(0);
      if (lane == 0) bst4(1u, rs, 0, uni(F_RDY + (64 + j) * 4));
    }
    unsigned offp[4];
#pragma unroll
    for (int i = 0; i < 4; ++i) offp[i] = poff(rep, (8 * w + 2 * i + (c16 >> 3)) * 640 + g * 128 + (c16 & 7) * 16);
    const unsigned uo = UF_OFF + (j * 32 + 8 * w) * 1024 + (g * 16 + pc) * 16;
    int known = 0;
    bf16x8 U[8];
    auto prefetch_u = [&](int s) {
      for (int spins = 0; known <= s && spins < SPIN_CAP; ++spins) {
        asm volatile("" ::: "memory");
        const int ss = min(s + (lane >> 3), NSEG - 1);
        unsigned v = bld4(rs, F_U + (ss * 32 + 8 * w + (lane & 7)) * 4, 0);
        known = s + ready_upto(__builtin_amdgcn_ballot_w64(v == SENT), 3);
        if (known <= s) __builtin_amdgcn_s_sleep(4);
      }
      asm volatile("" ::: "memory");
#pragma unroll
      for (int i = 0; i < 8; ++i) U[i] = bldf(rs, uo, uni(s * 40 * 32768 + i * 1024));
    };
    for (int s = 0; s < NSEG - 1; ++s) {
      const unsigned sb = slotbase(s, par), sb1 = slotbase(s + 1, par);
      if (s > 0) __builtin_amdgcn_s_sleep(UDELAY);
      prefetch_u(s);
      __builtin_amdgcn_s_barrier();
      f32x4 lo, hi;
      for (int spins = 0; spins < SPIN_CAP; ++spins) {
        asm volatile("" ::: "memory");
        u32x4 vp[4];
#pragma unroll
        for (int i = 0; i < 4; ++i) vp[i] = bld16(rs, offp[i], sb);
        lo = (f32x4){}; hi = (f32x4){};
        unsigned m = 0;
#pragma unroll
        for (int i = 0; i < 4; ++i) {
          lo = mfma16(U[2 * i], __builtin_bit_cast(bf16x8, vp[i]), lo);
          hi = mfma16(U[2 * i + 1], __builtin_bit_cast(bf16x8, vp[i]), hi);
          m = max(m, umax4(vp[i]));
          SB();
        }
        if (__builtin_amdgcn_ballot_w64(m == SENT) == 0) break;
      }
      f32x4 sel = (lane & 8) ? hi : lo;
      if (w != 0) ldswrite16(red + (s & 1) * 8192 + (w * 64 + lane) * 16, __builtin_bit_cast(u32x4, sel));
      BAR();
      if (w == 0) {
        f32x4 t[7];
#pragma unroll
        for (int i = 0; i < 7; ++i) t[i] = *(LAS f32x4*)(uintptr_t)(red + (s & 1) * 8192 + ((i + 1) * 64 + lane) * 16);
        SB();
        f32x4 a = ((sel + t[0]) + (t[1] + t[2])) + ((t[3] + t[4]) + (t[5] + t[6]));
#pragma unroll
        for (int i = 0; i < 4; ++i) a[i] += ror8(a[i]);
        const u32x2 mv = {nosent(pk2(a[0], a[1])), nosent(pk2(a[2], a[3]))};
        bst8(mv, rs, mo, sb1);
        if (s + PAHEAD < NSEG) { bst8((u32x2){SENT, SENT}, rs, mo, slotbase(s + PAHEAD, par)); }
      }
    }
  } else {
    unsigned offy[2], offm[1];
#pragma unroll
    for (int i = 0; i < 2; ++i) offy[i] = yoff(j, (8 * w3 + 4 * i + g) * 256 + pc * 16);
    offm[0] = poff(rep, (8 * w3 + 4 * (c16 >> 3) + g) * 640 + 512 + (c16 & 7) * 16);
    if (w == 4) wait_flags(rs, F_RDY + lane * 4);
    for (int s = 0; s < NSEG - 1; ++s) {
      const unsigned sb = slotbase(s, par);
      u32x4 vy[3];
      const unsigned off3[3] = {offy[0], offy[1], offm[0]};
      if (w == 4) pace(rs, sb + bq, 0);
      __builtin_amdgcn_s_barrier();
      f32x4 lo, hi;
      for (int spins = 0; spins < SPIN_CAP; ++spins) {
        asm volatile("" ::: "memory");
        vy[2] = bld16(rs, off3[2], sb);
        vy[0] = bld16(rs, off3[0], sb); vy[1] = bld16(rs, off3[1], sb);
        lo = mfma16(__builtin_bit_cast(bf16x8, vy[0]), __builtin_bit_cast(bf16x8, vy[2]), (f32x4){});
        unsigned m = max(umax4(vy[2]), umax4(vy[0]));
        SB();
        hi = mfma16(__builtin_bit_cast(bf16x8, vy[1]), __builtin_bit_cast(bf16x8, vy[2]), (f32x4){});
        m = max(m, umax4(vy[1]));
        if (__builtin_amdgcn_ballot_w64(m == SENT) == 0) break;
      }
      f32x4 sel = (lane & 8) ? hi : lo;
      ldswrite16(red + (s & 1) * 8192 + (w * 64 + lane) * 16, __builtin_bit_cast(u32x4, sel));
      BAR();
    }
  }
}

DEV void c2_task(const Params& p, __amdgpu_buffer_rsrc_t rs, unsigned long long par, int s, int cg, unsigned lb, int w, int lane) {
  const int g = lane >> 4, c16 = lane & 15;
  const int j = cg * 8 + w;
  wait_flags(rs, F_U + (s * 32 + (lane & 31)) * 4);
  bf16x8 U[32], Y[8];
  const unsigned so = uni((s * 40 + j) * 32768);
#pragma unroll
  for (int h = 0; h < 32; ++h) U[h] = bldf(rs, lane * 16, so + UF_OFF + h * 1024);
  wait_flags(rs, F_S + (lane < 2 ? 2 * j + lane : 80 + (s >> 2)) * 4);
  float gt[2]; unsigned short bb[2][4];
#pragma unroll
  for (int tt = 0; tt < 2; ++tt) {
    const int t = 32 * s + 16 * tt + c16;
    gt[tt] = __builtin_bit_cast(float, bld4(rs, G_OFF + t * 4, 0));
#pragma unroll
    for (int i = 0; i < 4; ++i) bb[tt][i] = __builtin_amdgcn_raw_buffer_load_b16(rs, BM_OFF + ((16 * j + 4 * g + i) * SEQ + t) * 2, 0, 16);
  }
  wait_flags(rs, F_PROG + (s + 2) * 4);
  {
    u32x4 t[10];
#pragma unroll
    for (int i = 0; i < 8; ++i) t[i] = bld16(rs, lane * 16, uni(PX_OFF + s * 65536 + (w * 8 + i) * 1024));
#pragma unroll
    for (int i = 0; i < 2; ++i) t[8 + i] = bld16(rs, lane * 16, uni(PM_OFF + s * 16384 + (w * 2 + i) * 1024));
    BAR();
#pragma unroll
    for (int i = 0; i < 8; ++i) ldswrite16(lb + (w * 8 + i) * 1024 + lane * 16, t[i]);
#pragma unroll
    for (int i = 0; i < 2; ++i) ldswrite16(lb + 65536 + (w * 2 + i) * 1024 + lane * 16, t[8 + i]);
  }
  BAR();
#pragma unroll
  for (int hq = 0; hq < 8; ++hq) Y[hq] = bldf(rs, lane * 16, slotbase(s, par) + uni(yoff(j, 0) + hq * 1024));
  f32x4 a0 = {}, a1 = {};
#pragma unroll
  for (int h = 0; h < 32; ++h) {
    a0 = mfma16(U[h], ldsread16(lb + (h * 2) * 1024 + lane * 16), a0);
    a1 = mfma16(U[h], ldsread16(lb + (h * 2 + 1) * 1024 + lane * 16), a1);
  }
#pragma unroll
  for (int hq = 0; hq < 8; ++hq) {
    a0 = mfma16(Y[hq], ldsread16(lb + 65536 + (hq * 2) * 1024 + lane * 16), a0);
    a1 = mfma16(Y[hq], ldsread16(lb + 65536 + (hq * 2 + 1) * 1024 + lane * 16), a1);
  }
#pragma unroll
  for (int tt = 0; tt < 2; ++tt) {
    const f32x4 a = tt ? a1 : a0;
    const float og = bfr(1.0f - gt[tt]);
    const float bv[4] = {bflo(bb[tt][0]), bflo(bb[tt][1]), bflo(bb[tt][2]), bflo(bb[tt][3])};
    float o[4];
#pragma unroll
    for (int i = 0; i < 4; ++i) o[i] = bfr(gt[tt] * bfr(a[i])) + bfr(og * bv[i]);
    ldswrite8(lb + 81920 + (16 * tt + c16) * 272 + w * 32 + g * 8, (u32x2){pk2(o[0], o[1]), pk2(o[2], o[3])});
  }
  BAR();
  {
    const int row = threadIdx.x >> 4, pc = threadIdx.x & 15;
    const u32x4 v = *(LAS u32x4*)(uintptr_t)(lb + 81920 + row * 272 + pc * 16);
    bst16(v, mkrsrc(p.out), ((32 * s + row) * DM + cg * 128) * 2 + pc * 16, 0);
  }
}

template <int NT, bool DIRECT>
DEV void fine_task(const Params& p, __amdgpu_buffer_rsrc_t rs, unsigned long long par, int s, int j, int tt0, unsigned lb, int w, int lane) {
  const int g = lane >> 4, c16 = lane & 15;
  wait_flags(rs, F_U + (s * 32 + 4 * w + (lane & 3)) * 4);
  bf16x8 U[4];
#pragma unroll
  for (int i = 0; i < 4; ++i) U[i] = bldf(rs, lane * 16, uni(UF_OFF + ((s * 40 + j) * 32 + 4 * w + i) * 1024));
  float gt = 0.f; unsigned short bb[4] = {};
  const int t = 32 * s + 16 * (tt0 + (w & (NT - 1))) + c16;
  if (w < NT) {
    wait_flags(rs, F_S + (lane < 2 ? 2 * j + lane : 80 + (s >> 2)) * 4);
    gt = __builtin_bit_cast(float, bld4(rs, G_OFF + t * 4, 0));
#pragma unroll
    for (int i = 0; i < 4; ++i) bb[i] = __builtin_amdgcn_raw_buffer_load_b16(rs, BM_OFF + ((16 * j + 4 * g + i) * SEQ + t) * 2, 0, 16);
  }
  wait_flags(rs, F_PROG + (s - 1) * 4);
  wait_flags<2>(rs, F_PROG + s * 4);
  if (DIRECT) pace(rs, slotbase(s, par) + uni(memoff(0, BELL_OFF)), TF - TP);
  const unsigned yo = slotbase(s, par) + uni(yoff(j, 0) + w * 1024);
  f32x4 acc[NT];
  for (int spins = 0; spins < SPIN_CAP; ++spins) {
    asm volatile("" ::: "memory");
    u32x4 px[NT][4], pm[NT];
    const u32x4 y = bld16(rs, lane * 16, yo);
#pragma unroll
    for (int q = 0; q < NT; ++q) {
      pm[q] = bld16(rs, lane * 16, uni(PM_OFF + ((s * 8 + w) * 2 + tt0 + q) * 1024));
#pragma unroll
      for (int i = 0; i < 4; ++i) px[q][i] = bld16(rs, lane * 16, uni(PX_OFF + ((s * 32 + 4 * w + i) * 2 + tt0 + q) * 1024));
    }
    unsigned m = umax4(y);
#pragma unroll
    for (int q = 0; q < NT; ++q) {
      acc[q] = mfma16(__builtin_bit_cast(bf16x8, y), __builtin_bit_cast(bf16x8, pm[q]), (f32x4){});
      m = max(m, umax4(pm[q]));
#pragma unroll
      for (int i = 0; i < 4; ++i) { acc[q] = mfma16(U[i], __builtin_bit_cast(bf16x8, px[q][i]), acc[q]); m = max(m, umax4(px[q][i])); }
    }
    if (!DIRECT || __builtin_amdgcn_ballot_w64(m == SENT) == 0) break;
  }
#pragma unroll
  for (int q = 0; q < NT; ++q) ldswrite16(lb + ((q * 8 + w) * 64 + lane) * 16, __builtin_bit_cast(u32x4, acc[q]));
  BAR();
  if (w < NT) {
    f32x4 a = {};
#pragma unroll
    for (int i = 0; i < 8; ++i) a += *(LAS f32x4*)(uintptr_t)(lb + ((w * 8 + i) * 64 + lane) * 16);
    const float og = bfr(1.0f - gt);
    float o[4];
#pragma unroll
    for (int i = 0; i < 4; ++i) o[i] = bfr(gt * bfr(a[i])) + bfr(og * bflo(bb[i]));
    *(u32x2*)((char*)p.out + ((size_t)t * DM + 16 * j + 4 * g) * 2) = (u32x2){pk2(o[0], o[1]), pk2(o[2], o[3])};
  }
}
DEV void xf_task(const Params& p, __amdgpu_buffer_rsrc_t rs, int sg, int tid) {
  u32x4 v[5];
#pragma unroll
  for (int i = 0; i < 5; ++i) {
    const int c = i * 512 + tid, l = c & 63, q = c >> 6;
    v[i] = *(const u32x4*)(p.x + (size_t)(32 * sg + 16 * (q / 20) + (l & 15)) * DM + 32 * (q % 20) + 8 * (l >> 4));
  }
#pragma unroll
  for (int i = 0; i < 5; ++i) bst16(v[i], rs, tid * 16, uni(XF_OFF + sg * 40960 + i * 8192));
  WAIT_VM(0); BAR();
  if (tid == 0) bst4(1u, rs, 0, uni(F_X + sg * 4));
}

constexpr int XPAD = 272;
constexpr int XCOL_B = (SEQ + XPAD) * 2;
constexpr int KR_N = 2112;
constexpr int KR_X0 = 2080;
constexpr unsigned SS_XC = 0, SS_KR = 37120, SS_KT = SS_KR + 67584, SS_IX = SS_KT + 32768;
DEV bf16x8 toep(unsigned a, unsigned ash) {
  const LAS unsigned* q = (const LAS unsigned*)(uintptr_t)a;
  unsigned d0 = q[0], d1 = q[1], d2 = q[2], d3 = q[3], d4 = q[4];
  u32x4 o = {__builtin_amdgcn_alignbyte(d1, d0, ash), __builtin_amdgcn_alignbyte(d2, d1, ash), __builtin_amdgcn_alignbyte(d3, d2, ash), __builtin_amdgcn_alignbyte(d4, d3, ash)};
  return __builtin_bit_cast(bf16x8, o);
}
DEV int ssm_base(int rho) {
  return rho < 16 ? 128 * (rho >> 3) + (rho & 7) : rho < 24 ? 256 + 128 * ((rho - 16) >> 2) + 2 * (rho & 3)
       : rho < 32 ? 512 + 128 * ((rho - 24) >> 1) + 4 * (rho & 1) : rho <= 40 ? 1024 + 128 * (rho - 32) : 0;
}
DEV void ssm_setup(unsigned lb, int tid) {
  for (int x = tid; x < KR_N; x += 512) {
    const int l = KR_X0 - x;
    int o = 41 * 64;
    if (l >= 0 && l < SEQ) {
      const int lr = (int)bfr((float)l);
      const int blk = lr >> 7, e = lr & 7;
      const int rho = blk < 2 ? 8 * blk + e : blk < 4 ? 16 + 4 * (blk - 2) + (e >> 1) : blk < 8 ? 24 + 2 * (blk - 4) + (e >> 2) : 24 + blk;
      o = rho * 64 + ((lr >> 3) & 15) * 4;
    }
    *(LAS unsigned short*)(uintptr_t)(lb + SS_IX + x * 2) = (unsigned short)o;
  }
}
DEV void ssm_task(const Params& p, __amdgpu_buffer_rsrc_t rs, int cgp, unsigned lb, int w, int lane, int tid) {
  const int d0 = cgp * 8, d = d0 + w;
  const unsigned xc = lb + SS_XC, kt = lb + SS_KT;
  const unsigned kh = lb + SS_KR + w * (KR_N * 4), kl = kh + KR_N * 2;
  const int c16 = lane & 15, kq = lane >> 4;
  const float dt = bfr(__expf((float)p.logdt[d]));
  const float dsk = (float)p.Dskip[d];
  const int po = d * 64 + lane;
  const unsigned ar = ((const unsigned*)p.Are)[po], ai = ((const unsigned*)p.Aim)[po], br = ((const unsigned*)p.Bre)[po], bi = ((const unsigned*)p.Bim)[po],
                 cr = ((const unsigned*)p.Cre)[po], ci = ((const unsigned*)p.Cim)[po];
  BAR();
#pragma unroll
  for (int e = 0; e < 2; ++e) {
    const float are = dt * (e ? bfhi(ar) : bflo(ar)), aim = dt * (e ? bfhi(ai) : bflo(ai));
    const float bre = dt * (e ? bfhi(br) : bflo(br)), bim = dt * (e ? bfhi(bi) : bflo(bi));
    const float cre = e ? bfhi(cr) : bflo(cr), cim = e ? bfhi(ci) : bflo(ci);
    const float zr = fmaf(cre, bre, -cim * bim), zi = fmaf(cre, bim, cim * bre);
    const float i2p_hi = 0.15915494309189535f, i2p_lo = 6.4206382e-09f;
    const float wf = aim * i2p_hi;
    const float whi_ = __builtin_bit_cast(float, __builtin_bit_cast(unsigned, wf) & 0xffffff00u);
    const float wlo_ = fmaf(aim, i2p_lo, fmaf(aim, i2p_hi, -whi_));
    const float mxc = fmaxf(fabsf(zr), fabsf(zi));
    const int ex = __builtin_amdgcn_frexp_expf(mxc);
    const float sr = __builtin_amdgcn_ldexpf(zr, -ex), si = __builtin_amdgcn_ldexpf(zi, -ex);
    const float lc = 0.5f * __builtin_amdgcn_logf(fmaf(sr, sr, si * si)) + (float)ex;
    *(LAS f32x4*)(uintptr_t)(kh + (2 * lane + e) * 16) = (f32x4){are * 1.4426950408889634f, whi_, wlo_, lc};
    *(LAS float*)(uintptr_t)(kh + 2048 + (2 * lane + e) * 4) = atan2f(zi, zr) * i2p_hi;
  }
  for (int i = lane; i < XPAD / 2; i += 64) *(LAS unsigned*)(uintptr_t)(xc + w * XCOL_B + i * 4) = 0u;
  WAIT_LGKM0();
  f32x4 T0 = {}, T1 = {}, T2 = {};
  const float lr_ = (float)(8 * c16), l0 = (float)ssm_base(c16), l1 = (float)ssm_base(16 + c16), l2 = (float)ssm_base(32 + c16);
  u32x4 xv[4];
#pragma unroll 2
  for (int u = 0; u < 32; ++u) {
    if (u == 24) {
#pragma unroll
      for (int i = 0; i < 4; ++i) xv[i] = *(const u32x4*)(p.x + (size_t)(tid + 512 * i) * DM + d0);
    }
    const f32x4 pr = *(LAS f32x4*)(uintptr_t)(kh + (4 * u + kq) * 16);
    const float ph = *(LAS float*)(uintptr_t)(kh + 2048 + (4 * u + kq) * 4);
    const float mr = __builtin_amdgcn_exp2f(pr[0] * lr_), fr = __builtin_amdgcn_fractf(fmaf(pr[2], lr_, __builtin_amdgcn_fractf(pr[1] * lr_)));
    const float Rre = mr * __builtin_amdgcn_cosf(fr), Rim = -mr * __builtin_amdgcn_sinf(fr);
    const float m0 = __builtin_amdgcn_exp2f(fmaf(pr[0], l0, pr[3])), f0 = __builtin_amdgcn_fractf(__builtin_amdgcn_fractf(pr[1] * l0) + fmaf(pr[2], l0, ph));
    const float m1 = __builtin_amdgcn_exp2f(fmaf(pr[0], l1, pr[3])), f1 = __builtin_amdgcn_fractf(__builtin_amdgcn_fractf(pr[1] * l1) + fmaf(pr[2], l1, ph));
    const float m2 = __builtin_amdgcn_exp2f(fmaf(pr[0], l2, pr[3])), f2 = __builtin_amdgcn_fractf(__builtin_amdgcn_fractf(pr[1] * l2) + fmaf(pr[2], l2, ph));
    T0 = __builtin_amdgcn_mfma_f32_16x16x4f32(m0 * __builtin_amdgcn_cosf(f0), Rre, T0, 0, 0, 0);
    T1 = __builtin_amdgcn_mfma_f32_16x16x4f32(m1 * __builtin_amdgcn_cosf(f1), Rre, T1, 0, 0, 0);
    T2 = __builtin_amdgcn_mfma_f32_16x16x4f32(m2 * __builtin_amdgcn_cosf(f2), Rre, T2, 0, 0, 0);
    T0 = __builtin_amdgcn_mfma_f32_16x16x4f32(m0 * __builtin_amdgcn_sinf(f0), Rim, T0, 0, 0, 0);
    T1 = __builtin_amdgcn_mfma_f32_16x16x4f32(m1 * __builtin_amdgcn_sinf(f1), Rim, T1, 0, 0, 0);
    T2 = __builtin_amdgcn_mfma_f32_16x16x4f32(m2 * __builtin_amdgcn_sinf(f2), Rim, T2, 0, 0, 0);
  }
#pragma unroll
  for (int i = 0; i < 4; ++i) ldswrite16(kt + (tid + 512 * i) * 16, xv[i]);
  BAR();
#pragma unroll
  for (int i = 0; i < 8; ++i) {
    const int t0 = (w * 8 + i) * 32 + kq * 8;
    const bf16x4 v = trread(kt + (t0 + (c16 >> 2) + 4 * ((lane >> 1) & 1)) * 16 + (lane & 1) * 8);
    ldswrite8(xc + (c16 & 7) * XCOL_B + (XPAD + t0 + 4 * (c16 >> 3)) * 2, __builtin_bit_cast(u32x2, v));
  }
  BAR();
  const unsigned tb = kt + w * 3072;
#pragma unroll
  for (int i = 0; i < 4; ++i) {
    const unsigned o = ((4 * kq + i) * 16 + c16) * 4;
    *(LAS float*)(uintptr_t)(tb + o) = T0[i]; *(LAS float*)(uintptr_t)(tb + 1024 + o) = T1[i];
    *(LAS float*)(uintptr_t)(tb + 2048 + o) = (4 * kq + i > 8) ? 0.f : T2[i];
  }
  WAIT_LGKM0();
#pragma unroll 4
  for (int x2 = lane; x2 < KR_N / 2; x2 += 64) {
    const unsigned ix = *(LAS unsigned*)(uintptr_t)(lb + SS_IX + x2 * 4);
    const float v0 = *(LAS float*)(uintptr_t)(tb + (ix & 0xffff)), v1 = *(LAS float*)(uintptr_t)(tb + (ix >> 16));
    const unsigned hi = pk2(v0, v1);
    *(LAS unsigned*)(uintptr_t)(kh + x2 * 4) = hi;
    *(LAS unsigned*)(uintptr_t)(kl + x2 * 4) = pk2(v0 - bflo(hi), v1 - bfhi(hi));
  }
  WAIT_LGKM0();
  f32x4 acc[8] = {};
  const int g = kq;
  const int xe = KR_X0 - 16 + 8 * g - c16;
  const unsigned ash = (xe & 1) * 2;
  const unsigned abase = kh + (unsigned)(xe >> 1) * 4;
  const unsigned bb = xc + w * XCOL_B + (XPAD - 16 + 16 * c16 + 8 * g) * 2;
  bf16x8 Ah = toep(abase, ash), Al = toep(abase + KR_N * 2, ash);
#pragma unroll
  for (int tq = 0; tq < 8; ++tq) {
#pragma unroll 1
    for (int r = 0; r < 8; ++r) {
      const int m = 16 * tq + 2 * r;
      bf16x8 B[8];
#pragma unroll
      for (int tau = tq; tau < 8; ++tau) B[tau] = ldsread16(bb + (16 * tau - m) * 32);
      const int mn = min(m + 2, 126);
      const bf16x8 Nh = toep(abase - mn * 32, ash), Nl = toep(abase + KR_N * 2 - mn * 32, ash);
#pragma unroll
      for (int tau = tq; tau < 8; ++tau) acc[tau] = mfma16(Ah, B[tau], acc[tau]);
#pragma unroll
      for (int tau = tq; tau < 8; ++tau) acc[tau] = mfma16(Al, B[tau], acc[tau]);
      Ah = Nh; Al = Nl;
    }
  }
#pragma unroll
  for (int tau = 0; tau < 8; ++tau) {
    const int t = 256 * tau + 16 * c16 + 4 * g;
    const u32x2 xx = *(LAS u32x2*)(uintptr_t)(xc + w * XCOL_B + (XPAD + t) * 2);
    const u32x2 o = {pk2(bfr(acc[tau][0]) + bfr(bflo(xx.x) * dsk), bfr(acc[tau][1]) + bfr(bfhi(xx.x) * dsk)),
                     pk2(bfr(acc[tau][2]) + bfr(bflo(xx.y) * dsk), bfr(acc[tau][3]) + bfr(bfhi(xx.y) * dsk))};
    bst8(o, rs, BM_OFF + (d * SEQ + t) * 2, 0);
  }
  WAIT_VM(0); BAR();
  if (tid == 0) bst4(1u, rs, F_S + cgp * 4, 0);
}
DEV void gate_task(const Params& p, __amdgpu_buffer_rsrc_t rs, int gi, int w, int lane, int tid) {
  const int c16 = lane & 15;
  u32x4 gv[5];
#pragma unroll
  for (int c = 0; c < 5; ++c) gv[c] = *(const u32x4*)(p.gatew + (c * 16 + c16) * 8);
#pragma unroll
  for (int ps = 0; ps < 4; ++ps) {
    const int t = gi * 128 + w * 16 + ps * 4 + (lane >> 4);
    float s = 0.f;
#pragma unroll
    for (int c = 0; c < 5; ++c) {
      const u32x4 xv = *(const u32x4*)(p.x + (size_t)t * DM + (c * 16 + c16) * 8);
#pragma unroll
      for (int i = 0; i < 4; ++i) s += bfr(bflo(xv[i]) * bflo(gv[c][i])) + bfr(bfhi(xv[i]) * bfhi(gv[c][i]));
    }
#pragma unroll
    for (int o = 1; o < 16; o <<= 1) s += __shfl_xor(s, o);
    s = bfr(s);
    if (c16 == 0) bst4(__builtin_bit_cast(unsigned, bfr(1.0f / (1.0f + __expf(-s)))), rs, G_OFF + t * 4, 0);
  }
  WAIT_VM(0); BAR();
  if (tid == 0) bst4(1u, rs, F_S + (80 + gi) * 4, 0);
}

DEV char* align_ws(char* ws) { return (char*)(((uintptr_t)ws + ((size_t)2 << 20) - 1) & ~(((uintptr_t)2 << 20) - 1)); }
__global__ void __launch_bounds__(64) k_init(Params p) {
  p.xch = align_ws(p.xch);
  const int i = blockIdx.x * 64 + threadIdx.x;
  if (i < (int)(CTL_BYTES / 16)) *(u32x4*)(p.xch + CTL_OFF + (size_t)i * 16) = (u32x4){SENT, SENT, SENT, SENT};
  if (i == 0) *(unsigned*)(p.xch + F_ROT) = (unsigned)__builtin_amdgcn_s_getreg(20 | (3 << 11)) & 7u;
}

__global__ void __launch_bounds__(512, 2) k_main(Params p) {
  extern __shared__ __attribute__((aligned(16))) char lds[];
  const unsigned lb = (unsigned)(uintptr_t)(LAS char*)lds;
  const int b = blockIdx.x, lane = threadIdx.x & 63, w = __builtin_amdgcn_readfirstlane(threadIdx.x >> 6);
  p.xch = align_ws(p.xch);
  const int xg = (b + (int)*(const unsigned*)(p.xch + F_ROT)) & 7, r = b >> 3;
  if (xg == 0) chain_qk(p, r, 0, lb, w, lane);
  else if (xg == 1) chain_vo(p, r, 1, lb, w, lane);
  else if (xg < 4 && r < 20) chain_r(p, 2 * r + (xg - 2), xg - 2, lb, w, lane);
  else {
    const __amdgpu_buffer_rsrc_t rs = mkrsrc(p.xch);
    int fid;
    if (xg >= 4 && r < 16) {
      if (r < 8) worker_qk(p, (xg - 4) * 8 + r, lb, w, lane);
      else worker_vo(p, (xg - 4) * 8 + (r - 8), lb, w, lane);
      fid = 32 + r * 4 + (xg - 4);
    } else {
      const int f = xg >= 4 ? (r - 16) * 4 + (xg - 4) : 64 + (r - 20) * 2 + (xg - 2);
      if (f < 64) xf_task(p, rs, f, threadIdx.x);
      if (f < 80) { ssm_setup(lb, threadIdx.x); ssm_task(p, rs, f, lb, w, lane, threadIdx.x); }
      else { gate_task(p, rs, 2 * (f - 80), w, lane, threadIdx.x); gate_task(p, rs, 2 * (f - 80) + 1, w, lane, threadIdx.x); }
      fid = f < 32 ? -1 : f < 64 ? f - 32 : f + 32;
      if (f < NC2) {
        const unsigned long long par = load_par(rs, lane);
        for (int t = f; t < (NSEG - NFINE) * 5; t += NC2) {
          c2_task(p, rs, par, t / 5, t % 5, lb, w, lane);
        }
      }
    }
    if (fid >= 0) {
      const unsigned long long par = load_par(rs, lane);
      if (fid < 80) fine_task<1, true>(p, rs, par, NSEG - 1, fid >> 1, fid & 1, lb, w, lane);
      else for (int s = NSEG - NFINE; s < NSEG - 1; ++s) fine_task<2, true>(p, rs, par, s, fid - 80, 0, lb + (s & 1) * 16384, w, lane);
    }
  }
}

extern "C" void kernel_launch(void* const* d_in, const int* in_sizes, int n_in, void* d_out, int out_size, void* d_ws, size_t ws_size,
                              hipStream_t stream) {
  Params p;
  p.x = (const bf16*)d_in[0]; p.mem0 = (const bf16*)d_in[1]; p.Wq = (const bf16*)d_in[2]; p.Wk = (const bf16*)d_in[3];
  p.Wv = (const bf16*)d_in[4]; p.Wo = (const bf16*)d_in[5];
  p.Are = (const bf16*)d_in[8]; p.Aim = (const bf16*)d_in[9]; p.Bre = (const bf16*)d_in[10]; p.Bim = (const bf16*)d_in[11];
  p.Cre = (const bf16*)d_in[12]; p.Cim = (const bf16*)d_in[13]; p.logdt = (const bf16*)d_in[14]; p.Dskip = (const bf16*)d_in[15];
  p.gatew = (const bf16*)d_in[16];
  p.out = (bf16*)d_out;
  p.xch = (char*)d_ws;
  const int LDS = 144 * 1024;
  (void)hipFuncSetAttribute((const void*)k_main, hipFuncAttributeMaxDynamicSharedMemorySize, LDS);
  hipLaunchKernelGGL(k_init, dim3(24), dim3(64), 0, stream, p);
  hipLaunchKernelGGL(k_main, dim3(256), dim3(512), LDS, stream, p);
}
```
